```python
import torch

def _softmax(x, axis=-1):
    return torch.softmax(x, dim=axis)

def _gelu(x):
    return torch.nn.functional.gelu(x, approximate='tanh')

def _silu(x):
    return torch.nn.functional.silu(x)

def _sigmoid(x):
    return torch.sigmoid(x)

def _layernorm(x, axis=-1, eps=1e-05):
    m = x.mean(dim=axis, keepdim=True)
    v = x.var(dim=axis, keepdim=True, unbiased=False)
    return (x - m) / torch.sqrt(v + eps)

def _scatter_add(tgt, idx, src, axis=0):
    return tgt.clone().index_add_(axis, idx, src)

def _segment_sum(src, seg, n):
    out = torch.zeros((n,) + src.shape[1:], dtype=src.dtype)
    return out.index_add_(0, seg, src)

def _pad(x, pw):
    flat = tuple((v for pair in reversed(pw) for v in pair))
    return torch.nn.functional.pad(x, flat)
kernel_name = 'standalone__dilated_atrous__vision'

def setup_inputs():

    _w = lambda a: (lambda t: t.to(torch.bfloat16) if t.is_floating_point() else t)(torch.as_tensor(a))
    H = W = 224
    C = 96
    k = 3
    return {'x': _w((lambda *s: torch.randn(*s, dtype=torch.bfloat16) * 0.05)(1, H, W, C)), 'w': _w((lambda *s: torch.randn(*s, dtype=torch.bfloat16) * 0.05)(k, k, C, C) * (1.0 / (k * C ** 0.5)))}

def reference(x, w):
    B, H, W, C = x.shape
    k = 3
    rate = 2
    pad = (k - 1) * rate // 2
    xp = _pad(x, ((0, 0), (pad, pad), (pad, pad), (0, 0)))
    out = torch.zeros_like(x)
    for i in range(k):
        for j in range(k):
            tap = xp[:, i * rate:i * rate + H, j * rate:j * rate + W, :].clone()
            out = out + torch.einsum('bhwc,co->bhwo', tap, w[i, j])
    return out
```

```cpp
#include <hip/hip_runtime.h>
#include <hip/hip_bf16.h>

__global__ void conv_naive(const __hip_bfloat16* __restrict__ x, const __hip_bfloat16* __restrict__ w, __hip_bfloat16* __restrict__ out) {
    int idx = blockIdx.x * blockDim.x + threadIdx.x;
    if (idx >= 224 * 224 * 96) return;
    int o = idx % 96;
    int p = idx / 96;
    int h = p / 224, ww = p % 224;
    float acc = 0.f;
    for (int i = 0; i < 3; ++i) {
        int hh = h + 2 * (i - 1);
        if (hh < 0 || hh >= 224) continue;
        for (int j = 0; j < 3; ++j) {
            int wc = ww + 2 * (j - 1);
            if (wc < 0 || wc >= 224) continue;
            const __hip_bfloat16* xp = x + (hh * 224 + wc) * 96;
            const __hip_bfloat16* wp = w + (i * 3 + j) * 96 * 96 + o;
            for (int c = 0; c < 96; ++c) acc += __bfloat162float(xp[c]) * __bfloat162float(wp[c * 96]);
        }
    }
    out[idx] = __float2bfloat16(acc);
}

extern "C" void kernel_launch(void* const* d_in, const int* in_sizes, int n_in,
                              void* d_out, int out_size, void* d_ws, size_t ws_size,
                              hipStream_t stream) {
    const __hip_bfloat16* x = (const __hip_bfloat16*)d_in[0];
    const __hip_bfloat16* w = (const __hip_bfloat16*)d_in[1];
    __hip_bfloat16* out = (__hip_bfloat16*)d_out;
    int n = 224 * 224 * 96;
    conv_naive<<<(n + 255) / 256, 256, 0, stream>>>(x, w, out);
}
```

```cpp
#include <hip/hip_runtime.h>
#include <hip/hip_bf16.h>

typedef short bf16x8 __attribute__((ext_vector_type(8)));
typedef short bf16x4 __attribute__((ext_vector_type(4)));
typedef float f32x4 __attribute__((ext_vector_type(4)));
typedef __attribute__((address_space(3))) void* lds_vp;
typedef __attribute__((address_space(3))) bf16x4* lds_v4p;

constexpr int IMG = 224;
constexpr int XPITCH = 7184;
constexpr int XBYTES = 9 * XPITCH;
constexpr int WSTAGE = 18432;
constexpr int NWBUF = 5;
constexpr int LDS_BYTES = XBYTES + NWBUF * WSTAGE;
constexpr unsigned OOB = 0x80000000u;

#define WAIT_V(n) asm volatile("s_waitcnt vmcnt(" #n ")" ::: "memory")
#define WAIT_L0() asm volatile("s_waitcnt lgkmcnt(0)" ::: "memory")
#define BAR() __builtin_amdgcn_s_barrier()
#define DMA16(ldsaddr, voff, rsrc, soff) \
    asm volatile("s_mov_b32 m0, %0\n\ts_nop 0\n\tbuffer_load_dwordx4 %1, %2, %3 offen lds" \
                 :: "s"(ldsaddr), "v"(voff), "s"(rsrc), "s"(soff) : "memory")
#define DMA16_LO32(ldsaddr, voff, rsrc, soff) \
    asm volatile("s_mov_b32 m0, %0\n\ts_mov_b64 exec, 0xffffffff\n\tbuffer_load_dwordx4 %1, %2, %3 offen lds\n\ts_mov_b64 exec, -1" \
                 :: "s"(ldsaddr), "v"(voff), "s"(rsrc), "s"(soff) : "memory")

__device__ __forceinline__ unsigned pack_bf16(float a, float b) {
    typedef __bf16 bf2 __attribute__((ext_vector_type(2)));
    bf2 v;
    v[0] = (__bf16)a;
    v[1] = (__bf16)b;
    return __builtin_bit_cast(unsigned, v);
}

__global__ __launch_bounds__(256, 1)
void conv_atrous(const unsigned short* __restrict__ x, const unsigned short* __restrict__ w,
                 unsigned short* __restrict__ out) {
    __shared__ __attribute__((aligned(16))) char smem[LDS_BYTES];

    const unsigned ldsb = (unsigned)(size_t)(lds_vp)smem;
    const int tid = threadIdx.x;
    const int lane = tid & 63;
    const int wid = __builtin_amdgcn_readfirstlane(tid >> 6);
    const int nh = wid & 1;
    const int mh = wid >> 1;

    const int bid = blockIdx.x;
    const int xcd = bid & 7, idx = bid >> 3;
    const int rb = 2 * xcd + idx / 14;
    const int rem = idx % 14;
    const int par = rem / 7, cb = rem % 7;

    auto xr = __builtin_amdgcn_make_buffer_rsrc((void*)x, 0, IMG * IMG * 192, 0x00020000);
    auto wr = __builtin_amdgcn_make_buffer_rsrc((void*)w, 0, 9 * 96 * 192, 0x00020000);

    unsigned xoff[2];
#pragma unroll
    for (int k = 0; k < 2; ++k) {
        int u = wid + 4 * k;
        int q = 64 * u + lane;
        int p = q / 12, s = q - 12 * p;
        int cg = s ^ ((p >> 2) & 3);
        int win = 32 * cb - 2 + p;
        bool ok = (q < 432) && (win >= 0) && (win < IMG);
        xoff[k] = ok ? (unsigned)(win * 192 + cg * 16) : OOB;
    }
    auto load_row = [&](int r) {
        int hr = 7 * rb + r - 1;
        unsigned soff = (hr >= 0 && hr < 112) ? (unsigned)((par + 2 * hr) * (IMG * 192)) : OOB;
        DMA16(ldsb + r * XPITCH + wid * 1024, xoff[0], xr, soff);
        if (wid < 3)
            DMA16(ldsb + r * XPITCH + (wid + 4) * 1024, xoff[1], xr, soff);
    };

    unsigned woff[5];
#pragma unroll
    for (int u = 0; u < 5; ++u) {
        int n = 288 * wid + 64 * u + lane;
        int i = n / 384, m = n - 384 * i;
        int c = m / 12, sc = m - 12 * c;
        int src = sc ^ (((c >> 3) & 1) << 1);
        woff[u] = (unsigned)(i * 55296 + c * 192 + src * 16);
    }
    auto load_w = [&](int s) {
        const int j = s / 3, ks = s % 3;
        const unsigned soff = j * 18432 + ks * 6144;
        unsigned base = ldsb + XBYTES + (s % NWBUF) * WSTAGE + wid * 4608;
#pragma unroll
        for (int u = 0; u < 4; ++u)
            DMA16(base + u * 1024, woff[u], wr, soff);
        DMA16_LO32(base + 4096, woff[4], wr, soff);
    };

#pragma unroll
    for (int r = 0; r < 6; ++r) load_row(r);
#pragma unroll
    for (int s = 0; s < 5; ++s) load_w(s);

    const int px = lane & 15, kg = lane >> 4;
    unsigned xa[3][2];
#pragma unroll
    for (int j = 0; j < 3; ++j) {
        int p = 16 * mh + px + 2 * j;
        int ch = kg ^ ((p >> 2) & 3);
#pragma unroll
        for (int hh = 0; hh < 2; ++hh)
            xa[j][hh] = (unsigned)(p * 192 + ch * 16 + 8 * (hh ^ (kg & 1)));
    }
    const int g = lane >> 4, q4 = (lane & 15) >> 2, p4 = lane & 3;
    unsigned wa[3][2];
#pragma unroll
    for (int t = 0; t < 3; ++t) {
#pragma unroll
        for (int tt = 0; tt < 2; ++tt) {
            int c = 8 * g + 4 * (tt ^ (g & 1)) + q4;
            int ch = (6 * nh + 2 * t + (p4 >> 1)) ^ ((g & 1) << 1);
            wa[t][tt] = (unsigned)(XBYTES + c * 192 + ch * 16 + (p4 & 1) * 8);
        }
    }

    bf16x8 Wf[9][3][3];
    f32x4 acc[7][3];
#pragma unroll
    for (int r = 0; r < 7; ++r)
#pragma unroll
        for (int t = 0; t < 3; ++t) acc[r][t] = (f32x4){0.f, 0.f, 0.f, 0.f};

    auto ldx = [&](int R, int j, int ks) -> bf16x8 {
        bf16x4 lo = *(const bf16x4*)(smem + xa[j][0] + R * XPITCH + ks * 64);
        bf16x4 hi = *(const bf16x4*)(smem + xa[j][1] + R * XPITCH + ks * 64);
        return __builtin_shufflevector(lo, hi, 0, 1, 2, 3, 4, 5, 6, 7);
    };

#pragma unroll
    for (int s = 0; s < 9; ++s) {
        const int j = s / 3, ks = s % 3;
        if (s == 0) { WAIT_V(20); }
        else if (s <= 5) { WAIT_V(15); }
        else if (s == 6) { WAIT_V(13); }
        else if (s == 7) { WAIT_V(8); }
        else { WAIT_V(3); }
        WAIT_L0();
        BAR();
        if (s >= 1 && s + 4 <= 8) load_w(s + 4);
        if (s == 5) {
            load_row(6); load_row(7); load_row(8);
        }
#pragma unroll
        for (int i = 0; i < 3; ++i)
#pragma unroll
            for (int t = 0; t < 3; ++t) {
                bf16x4 lo = __builtin_amdgcn_ds_read_tr16_b64_v4i16((lds_v4p)(smem + wa[t][0] + (s % NWBUF) * WSTAGE + i * 6144));
                bf16x4 hi = __builtin_amdgcn_ds_read_tr16_b64_v4i16((lds_v4p)(smem + wa[t][1] + (s % NWBUF) * WSTAGE + i * 6144));
                Wf[s][i][t] = __builtin_shufflevector(lo, hi, 0, 1, 2, 3, 4, 5, 6, 7);
            }
#pragma unroll
        for (int R = 0; R < 6; ++R) {
            bf16x8 xf = ldx(R, j, ks);
#pragma unroll
            for (int i = 0; i < 3; ++i) {
                const int r = R - i;
                if (r >= 0 && r < 4) {
#pragma unroll
                    for (int t = 0; t < 3; ++t)
                        acc[r][t] = __builtin_amdgcn_mfma_f32_16x16x32_bf16(Wf[s][i][t], xf, acc[r][t], 0, 0, 0);
                }
            }
        }
    }

    const int wcol = 32 * cb + 16 * mh + px;
    auto store_row = [&](int r) {
        int h = par + 2 * (7 * rb + r);
        unsigned short* o = out + (size_t)(h * IMG + wcol) * 96 + 48 * nh + 4 * kg;
#pragma unroll
        for (int t = 0; t < 3; ++t) {
            uint2 v;
            v.x = pack_bf16(acc[r][t][0], acc[r][t][1]);
            v.y = pack_bf16(acc[r][t][2], acc[r][t][3]);
            *(uint2*)(o + 16 * t) = v;
        }
    };

    WAIT_V(0);
    BAR();
#pragma unroll
    for (int r = 0; r < 4; ++r) store_row(r);

#pragma unroll
    for (int s = 0; s < 9; ++s) {
        const int j = s / 3, ks = s % 3;
#pragma unroll
        for (int R = 4; R < 9; ++R) {
            bf16x8 xf = ldx(R, j, ks);
#pragma unroll
            for (int i = 0; i < 3; ++i) {
                const int r = R - i;
                if (r >= 4 && r < 7) {
#pragma unroll
                    for (int t = 0; t < 3; ++t)
                        acc[r][t] = __builtin_amdgcn_mfma_f32_16x16x32_bf16(Wf[s][i][t], xf, acc[r][t], 0, 0, 0);
                }
            }
        }
    }
#pragma unroll
    for (int r = 4; r < 7; ++r) store_row(r);
}

extern "C" void kernel_launch(void* const* d_in, const int* in_sizes, int n_in,
                              void* d_out, int out_size, void* d_ws, size_t ws_size,
                              hipStream_t stream) {
    const unsigned short* x = (const unsigned short*)d_in[0];
    const unsigned short* w = (const unsigned short*)d_in[1];
    unsigned short* out = (unsigned short*)d_out;
    conv_atrous<<<224, 256, 0, stream>>>(x, w, out);
}
```

```cpp
#include <hip/hip_runtime.h>
#include <hip/hip_bf16.h>

typedef short bf16x8 __attribute__((ext_vector_type(8)));
typedef short bf16x4 __attribute__((ext_vector_type(4)));
typedef float f32x4 __attribute__((ext_vector_type(4)));
typedef __attribute__((address_space(3))) void* lds_vp;
typedef __attribute__((address_space(3))) bf16x4* lds_v4p;

constexpr int IMG = 224;
constexpr int XPITCH = 7184;
constexpr int XBYTES = 9 * XPITCH;
constexpr int WSTAGE = 18432;
constexpr int NWBUF = 4;
constexpr int PF = 3;
constexpr int LDS_BYTES = XBYTES + NWBUF * WSTAGE;
constexpr unsigned OOB = 0x80000000u;

#define WAIT_V(n) asm volatile("s_waitcnt vmcnt(" #n ")" ::: "memory")
#define WAIT_L0() asm volatile("s_waitcnt lgkmcnt(0)" ::: "memory")
#define BAR() __builtin_amdgcn_s_barrier()
#define DMA16(ldsaddr, voff, rsrc, soff) \
    asm volatile("s_mov_b32 m0, %0\n\ts_nop 0\n\tbuffer_load_dwordx4 %1, %2, %3 offen lds" \
                 :: "s"(ldsaddr), "v"(voff), "s"(rsrc), "s"(soff) : "memory")
#define DMA16_LO32(ldsaddr, voff, rsrc, soff) \
    asm volatile("s_mov_b32 m0, %0\n\ts_mov_b64 exec, 0xffffffff\n\tbuffer_load_dwordx4 %1, %2, %3 offen lds\n\ts_mov_b64 exec, -1" \
                 :: "s"(ldsaddr), "v"(voff), "s"(rsrc), "s"(soff) : "memory")

__device__ __forceinline__ unsigned pack_bf16(float a, float b) {
    typedef __bf16 bf2 __attribute__((ext_vector_type(2)));
    bf2 v;
    v[0] = (__bf16)a;
    v[1] = (__bf16)b;
    return __builtin_bit_cast(unsigned, v);
}

__global__ __launch_bounds__(256, 1)
void conv_atrous(const unsigned short* __restrict__ x, const unsigned short* __restrict__ w,
                 unsigned short* __restrict__ out) {
    __shared__ __attribute__((aligned(16))) char smem[LDS_BYTES];
    const unsigned ldsb = (unsigned)(size_t)(lds_vp)smem;
    const int tid = threadIdx.x;
    const int lane = tid & 63;
    const int wid = __builtin_amdgcn_readfirstlane(tid >> 6);
    const int nh = wid & 1;
    const int mh = wid >> 1;

    const int bid = blockIdx.x;
    const int xcd = bid & 7, idx = bid >> 3;
    const int rb = 2 * xcd + idx / 14;
    const int rem = idx % 14;
    const int par = rem / 7, cb = rem % 7;

    auto xr = __builtin_amdgcn_make_buffer_rsrc((void*)x, 0, IMG * IMG * 192, 0x00020000);
    auto wr = __builtin_amdgcn_make_buffer_rsrc((void*)w, 0, 9 * 96 * 192, 0x00020000);

    unsigned xoff[2];
#pragma unroll
    for (int k = 0; k < 2; ++k) {
        int u = wid + 4 * k;
        int q = 64 * u + lane;
        int p = q / 12, s = q - 12 * p;
        int cg = s ^ ((p >> 2) & 3);
        int win = 32 * cb - 2 + p;
        bool ok = (q < 432) && (win >= 0) && (win < IMG);
        xoff[k] = ok ? (unsigned)(win * 192 + cg * 16) : OOB;
    }
    auto load_row = [&](int r) {
        int hr = 7 * rb + r - 1;
        unsigned soff = (hr >= 0 && hr < 112) ? (unsigned)((par + 2 * hr) * (IMG * 192)) : OOB;
        DMA16(ldsb + r * XPITCH + wid * 1024, xoff[0], xr, soff);
        if (wid < 3)
            DMA16(ldsb + r * XPITCH + (wid + 4) * 1024, xoff[1], xr, soff);
    };

    unsigned woff[5];
#pragma unroll
    for (int u = 0; u < 5; ++u) {
        int n = 288 * wid + 64 * u + lane;
        int i = n / 384, m = n - 384 * i;
        int c = m / 12, sc = m - 12 * c;
        int src = sc ^ (((c >> 3) & 1) << 1);
        woff[u] = (unsigned)(i * 55296 + c * 192 + src * 16);
    }
    auto load_w = [&](int s) {
        const unsigned soff = (s < 9) ? (unsigned)(s * 6144) : OOB;
        unsigned base = ldsb + XBYTES + (s & (NWBUF - 1)) * WSTAGE + wid * 4608;
#pragma unroll
        for (int u = 0; u < 4; ++u)
            DMA16(base + u * 1024, woff[u], wr, soff);
        DMA16_LO32(base + 4096, woff[4], wr, soff);
    };

#pragma unroll
    for (int r = 0; r < 9; ++r) load_row(r);
#pragma unroll
    for (int s = 0; s < PF; ++s) load_w(s);

    const int px = lane & 15, kg = lane >> 4;
    const int g = lane >> 4, q4 = (lane & 15) >> 2, p4 = lane & 3;
    unsigned wa[3][2];
#pragma unroll
    for (int t = 0; t < 3; ++t) {
#pragma unroll
        for (int tt = 0; tt < 2; ++tt) {
            int c = 8 * g + 4 * (tt ^ (g & 1)) + q4;
            int ch = (6 * nh + 2 * t + (p4 >> 1)) ^ ((g & 1) << 1);
            wa[t][tt] = (unsigned)(XBYTES + c * 192 + ch * 16 + (p4 & 1) * 8);
        }
    }

    f32x4 acc[7][3];
#pragma unroll
    for (int r = 0; r < 7; ++r)
#pragma unroll
        for (int t = 0; t < 3; ++t) acc[r][t] = (f32x4){0.f, 0.f, 0.f, 0.f};

#pragma unroll 1
    for (int s = 0; s < 9; ++s) {
        const int j = s / 3, ks = s - 3 * j;
        WAIT_V(10);
        WAIT_L0();
        BAR();
        load_w(s + PF);
        const int p = 16 * mh + px + 2 * j;
        const unsigned xa0 = (unsigned)(p * 192 + (kg ^ ((p >> 2) & 3)) * 16 + 8 * (kg & 1) + ks * 64);
        const unsigned xa1 = xa0 ^ 8;
        const unsigned wb = (s & (NWBUF - 1)) * WSTAGE;
        bf16x8 Wf[3][3];
#pragma unroll
        for (int i = 0; i < 3; ++i)
#pragma unroll
            for (int t = 0; t < 3; ++t) {
                bf16x4 lo = __builtin_amdgcn_ds_read_tr16_b64_v4i16((lds_v4p)(smem + wa[t][0] + wb + i * 6144));
                bf16x4 hi = __builtin_amdgcn_ds_read_tr16_b64_v4i16((lds_v4p)(smem + wa[t][1] + wb + i * 6144));
                Wf[i][t] = __builtin_shufflevector(lo, hi, 0, 1, 2, 3, 4, 5, 6, 7);
            }
#pragma unroll
        for (int R = 0; R < 9; ++R) {
            bf16x4 lo = *(const bf16x4*)(smem + xa0 + R * XPITCH);
            bf16x4 hi = *(const bf16x4*)(smem + xa1 + R * XPITCH);
            bf16x8 xf = __builtin_shufflevector(lo, hi, 0, 1, 2, 3, 4, 5, 6, 7);
#pragma unroll
            for (int i = 0; i < 3; ++i) {
                const int r = R - i;
                if (r >= 0 && r < 7) {
#pragma unroll
                    for (int t = 0; t < 3; ++t)
                        acc[r][t] = __builtin_amdgcn_mfma_f32_16x16x32_bf16(Wf[i][t], xf, acc[r][t], 0, 0, 0);
                }
            }
        }
    }

    const int wcol = 32 * cb + 16 * mh + px;
#pragma unroll
    for (int r = 0; r < 7; ++r) {
        int h = par + 2 * (7 * rb + r);
        unsigned short* o = out + (size_t)(h * IMG + wcol) * 96 + 48 * nh + 4 * kg;
#pragma unroll
        for (int t = 0; t < 3; ++t) {
            uint2 v;
            v.x = pack_bf16(acc[r][t][0], acc[r][t][1]);
            v.y = pack_bf16(acc[r][t][2], acc[r][t][3]);
            *(uint2*)(o + 16 * t) = v;
        }
    }
    WAIT_V(0);
}

extern "C" void kernel_launch(void* const* d_in, const int* in_sizes, int n_in,
                              void* d_out, int out_size, void* d_ws, size_t ws_size,
                              hipStream_t stream) {
    const unsigned short* x = (const unsigned short*)d_in[0];
    const unsigned short* w = (const unsigned short*)d_in[1];
    unsigned short* out = (unsigned short*)d_out;
    conv_atrous<<<224, 256, 0, stream>>>(x, w, out);
}
```

```cpp
#include <hip/hip_runtime.h>
#include <hip/hip_bf16.h>

typedef short bf16x8 __attribute__((ext_vector_type(8)));
typedef short bf16x4 __attribute__((ext_vector_type(4)));
typedef float f32x4 __attribute__((ext_vector_type(4)));
typedef __attribute__((address_space(3))) void* lds_vp;
typedef __attribute__((address_space(3))) bf16x4* lds_v4p;

constexpr int IMG = 224;
constexpr int XPITCH = 7184;
constexpr int XBYTES = 9 * XPITCH;
constexpr int WSTAGE = 18432;
constexpr int NWBUF = 5;
constexpr int LDS_BYTES = XBYTES + NWBUF * WSTAGE;
constexpr unsigned OOB = 0x80000000u;

#define WAIT_V(n) asm volatile("s_waitcnt vmcnt(" #n ")" ::: "memory")
#define WAIT_L0() asm volatile("s_waitcnt lgkmcnt(0)" ::: "memory")
#define BAR() __builtin_amdgcn_s_barrier()
#define DMA16(ldsaddr, voff, rsrc, soff) \
    asm volatile("s_mov_b32 m0, %0\n\ts_nop 0\n\tbuffer_load_dwordx4 %1, %2, %3 offen lds" \
                 :: "s"(ldsaddr), "v"(voff), "s"(rsrc), "s"(soff) : "memory")
#define DMA16_LO32(ldsaddr, voff, rsrc, soff) \
    asm volatile("s_mov_b32 m0, %0\n\ts_mov_b64 exec, 0xffffffff\n\tbuffer_load_dwordx4 %1, %2, %3 offen lds\n\ts_mov_b64 exec, -1" \
                 :: "s"(ldsaddr), "v"(voff), "s"(rsrc), "s"(soff) : "memory")

__device__ __forceinline__ unsigned pack_bf16(float a, float b) {
    typedef __bf16 bf2 __attribute__((ext_vector_type(2)));
    bf2 v;
    v[0] = (__bf16)a;
    v[1] = (__bf16)b;
    return __builtin_bit_cast(unsigned, v);
}

__global__ __launch_bounds__(256, 1)
void conv_atrous(const unsigned short* __restrict__ x, const unsigned short* __restrict__ w,
                 unsigned short* __restrict__ out) {
    __shared__ __attribute__((aligned(16))) char smem[LDS_BYTES];

    const unsigned ldsb = (unsigned)(size_t)(lds_vp)smem;
    const int tid = threadIdx.x;
    const int lane = tid & 63;
    const int wid = __builtin_amdgcn_readfirstlane(tid >> 6);
    const int nh = wid & 1;
    const int mh = wid >> 1;

    const int bid = blockIdx.x;
    const int xcd = bid & 7, idx = bid >> 3;
    const int rb = 2 * xcd + idx / 14;
    const int rem = idx % 14;
    const int par = rem / 7, cb = rem % 7;

    auto xr = __builtin_amdgcn_make_buffer_rsrc((void*)x, 0, IMG * IMG * 192, 0x00020000);
    auto wr = __builtin_amdgcn_make_buffer_rsrc((void*)w, 0, 9 * 96 * 192, 0x00020000);

    unsigned xoff[2];
#pragma unroll
    for (int k = 0; k < 2; ++k) {
        int u = wid + 4 * k;
        int q = 64 * u + lane;
        int p = q / 12, s = q - 12 * p;
        int cg = s ^ ((p >> 2) & 3);
        int win = 32 * cb - 2 + p;
        bool ok = (q < 432) && (win >= 0) && (win < IMG);
        xoff[k] = ok ? (unsigned)(win * 192 + cg * 16) : OOB;
    }
    auto load_row = [&](int r) {
        int hr = 7 * rb + r - 1;
        unsigned soff = (hr >= 0 && hr < 112) ? (unsigned)((par + 2 * hr) * (IMG * 192)) : OOB;
        DMA16(ldsb + r * XPITCH + wid * 1024, xoff[0], xr, soff);
        if (wid < 3)
            DMA16(ldsb + r * XPITCH + (wid + 4) * 1024, xoff[1], xr, soff);
    };

    unsigned woff[5];
#pragma unroll
    for (int u = 0; u < 5; ++u) {
        int n = 288 * wid + 64 * u + lane;
        int i = n / 384, m = n - 384 * i;
        int c = m / 12, sc = m - 12 * c;
        int src = sc ^ (((c >> 3) & 1) << 1);
        woff[u] = (unsigned)(i * 55296 + c * 192 + src * 16);
    }
    auto load_w = [&](int s) {
        const int j = s / 3, ks = s % 3;
        const unsigned soff = j * 18432 + ks * 6144;
        unsigned base = ldsb + XBYTES + (s % NWBUF) * WSTAGE + wid * 4608;
#pragma unroll
        for (int u = 0; u < 4; ++u)
            DMA16(base + u * 1024, woff[u], wr, soff);
        DMA16_LO32(base + 4096, woff[4], wr, soff);
    };

#pragma unroll
    for (int r = 0; r < 6; ++r) load_row(r);
#pragma unroll
    for (int s = 0; s < 5; ++s) load_w(s);

    const int px = lane & 15, kg = lane >> 4;
    unsigned xa[3][2];
#pragma unroll
    for (int j = 0; j < 3; ++j) {
        int p = 16 * mh + px + 2 * j;
        int ch = kg ^ ((p >> 2) & 3);
#pragma unroll
        for (int hh = 0; hh < 2; ++hh)
            xa[j][hh] = (unsigned)(p * 192 + ch * 16 + 8 * (hh ^ (kg & 1)));
    }
    const int g = lane >> 4, q4 = (lane & 15) >> 2, p4 = lane & 3;
    unsigned wa[3][2];
#pragma unroll
    for (int t = 0; t < 3; ++t) {
#pragma unroll
        for (int tt = 0; tt < 2; ++tt) {
            int c = 8 * g + 4 * (tt ^ (g & 1)) + q4;
            int ch = (6 * nh + 2 * t + (p4 >> 1)) ^ ((g & 1) << 1);
            wa[t][tt] = (unsigned)(XBYTES + c * 192 + ch * 16 + (p4 & 1) * 8);
        }
    }

    bf16x8 Wf[9][3][3];
    f32x4 acc[7][3];
#pragma unroll
    for (int r = 0; r < 7; ++r)
#pragma unroll
        for (int t = 0; t < 3; ++t) acc[r][t] = (f32x4){0.f, 0.f, 0.f, 0.f};

    auto ldx = [&](int R, int j, int ks) -> bf16x8 {
        bf16x4 lo = *(const bf16x4*)(smem + xa[j][0] + R * XPITCH + ks * 64);
        bf16x4 hi = *(const bf16x4*)(smem + xa[j][1] + R * XPITCH + ks * 64);
        return __builtin_shufflevector(lo, hi, 0, 1, 2, 3, 4, 5, 6, 7);
    };

#pragma unroll
    for (int s = 0; s < 9; ++s) {
        const int j = s / 3, ks = s % 3;
        if (s == 0) { WAIT_V(20); }
        else if (s <= 5) { WAIT_V(15); }
        else if (s == 6) { WAIT_V(13); }
        else if (s == 7) { WAIT_V(8); }
        else { WAIT_V(3); }
        WAIT_L0();
        BAR();
        if (s >= 1 && s + 4 <= 8) load_w(s + 4);
        if (s == 5) {
            load_row(6); load_row(7); load_row(8);
        }
#pragma unroll
        for (int i = 0; i < 3; ++i)
#pragma unroll
            for (int t = 0; t < 3; ++t) {
                bf16x4 lo = __builtin_amdgcn_ds_read_tr16_b64_v4i16((lds_v4p)(smem + wa[t][0] + (s % NWBUF) * WSTAGE + i * 6144));
                bf16x4 hi = __builtin_amdgcn_ds_read_tr16_b64_v4i16((lds_v4p)(smem + wa[t][1] + (s % NWBUF) * WSTAGE + i * 6144));
                Wf[s][i][t] = __builtin_shufflevector(lo, hi, 0, 1, 2, 3, 4, 5, 6, 7);
            }
#pragma unroll
        for (int R = 0; R < 6; ++R) {
            bf16x8 xf = ldx(R, j, ks);
#pragma unroll
            for (int i = 0; i < 3; ++i) {
                const int r = R - i;
                if (r >= 0 && r < 4) {
#pragma unroll
                    for (int t = 0; t < 3; ++t)
                        acc[r][t] = __builtin_amdgcn_mfma_f32_16x16x32_bf16(Wf[s][i][t], xf, acc[r][t], 0, 0, 0);
                }
            }
        }
    }

    const int wcol = 32 * cb + 16 * mh + px;
    auto store_row = [&](int r) {
        int h = par + 2 * (7 * rb + r);
        unsigned short* o = out + (size_t)(h * IMG + wcol) * 96 + 48 * nh + 4 * kg;
#pragma unroll
        for (int t = 0; t < 3; ++t) {
            uint2 v;
            v.x = pack_bf16(acc[r][t][0], acc[r][t][1]);
            v.y = pack_bf16(acc[r][t][2], acc[r][t][3]);
            *(uint2*)(o + 16 * t) = v;
        }
    };

    WAIT_V(0);
    BAR();
#pragma unroll
    for (int r = 0; r < 4; ++r) store_row(r);

#pragma unroll
    for (int s = 0; s < 9; ++s) {
        const int j = s / 3, ks = s % 3;
#pragma unroll
        for (int R = 4; R < 9; ++R) {
            bf16x8 xf = ldx(R, j, ks);
#pragma unroll
            for (int i = 0; i < 3; ++i) {
                const int r = R - i;
                if (r >= 4 && r < 7) {
#pragma unroll
                    for (int t = 0; t < 3; ++t)
                        acc[r][t] = __builtin_amdgcn_mfma_f32_16x16x32_bf16(Wf[s][i][t], xf, acc[r][t], 0, 0, 0);
                }
            }
        }
    }
#pragma unroll
    for (int r = 4; r < 7; ++r) store_row(r);
}

__global__ __launch_bounds__(256) void prefetch_k(const uint4* __restrict__ x, const uint4* __restrict__ w) {
    const int n = IMG * IMG * 192 / 16;
    unsigned acc = 0;
    for (int i = blockIdx.x * 256 + threadIdx.x; i < n; i += gridDim.x * 256) { uint4 v = x[i]; acc ^= v.x ^ v.y ^ v.z ^ v.w; }
    const int m = 9 * 96 * 192 / 16;
    for (int i = blockIdx.x * 256 + threadIdx.x; i < m; i += gridDim.x * 256) { uint4 v = w[i]; acc ^= v.x ^ v.y ^ v.z ^ v.w; }
    asm volatile("" :: "v"(acc));
}

extern "C" void kernel_launch(void* const* d_in, const int* in_sizes, int n_in,
                              void* d_out, int out_size, void* d_ws, size_t ws_size,
                              hipStream_t stream) {
    const unsigned short* x = (const unsigned short*)d_in[0];
    const unsigned short* w = (const unsigned short*)d_in[1];
    unsigned short* out = (unsigned short*)d_out;
    prefetch_k<<<1024, 256, 0, stream>>>((const uint4*)x, (const uint4*)w);
    conv_atrous<<<224, 256, 0, stream>>>(x, w, out);
}
```

```cpp
#include <hip/hip_runtime.h>
#include <hip/hip_bf16.h>

typedef __attribute__((address_space(3))) void* lds_vp;

constexpr unsigned IMG = 224;
constexpr unsigned XPITCH = 6928;
constexpr unsigned WSTAGE = 18432;
constexpr unsigned WBYTES = 92160;
constexpr unsigned LDS_BYTES = 155536;
constexpr unsigned OOB = 0x80000000u;

__global__ __launch_bounds__(256, 1)
void conv_atrous(const unsigned short* __restrict__ x, const unsigned short* __restrict__ w,
                 unsigned short* __restrict__ out) {
    __shared__ __attribute__((aligned(16))) char smem[LDS_BYTES];
    const unsigned ldsb = (unsigned)(size_t)(lds_vp)smem;
    const unsigned tid = threadIdx.x;
    const unsigned lane = tid & 63;
    const unsigned wid = __builtin_amdgcn_readfirstlane(tid >> 6);
    const unsigned nh = wid & 1;
    const unsigned mh = wid >> 1;

    const unsigned bid = blockIdx.x;
    const unsigned xcd = bid & 7, idx = bid >> 3;
    const unsigned hi14 = idx >= 14 ? 1u : 0u;
    const unsigned rb = 2 * xcd + hi14;
    const unsigned rem = idx - 14 * hi14;
    const unsigned par = rem >= 7 ? 1u : 0u, cb = rem - 7 * par;

    auto xr = __builtin_amdgcn_make_buffer_rsrc((void*)x, 0, IMG * IMG * 192, 0x00020000);
    auto wr = __builtin_amdgcn_make_buffer_rsrc((void*)w, 0, 9 * 96 * 192, 0x00020000);

    unsigned xo[2];
#pragma unroll
    for (unsigned k = 0; k < 2; ++k) {
        unsigned q = 108 * wid + 64 * k + lane;
        unsigned p = (q * 43691u) >> 19, s = q - 12 * p;
        unsigned cg = s ^ ((p >> 2) & 3);
        unsigned win = 32 * cb - 2 + p;
        xo[k] = (win < IMG) ? (win * 192 + cg * 16) : OOB;
    }
    const unsigned xb = __builtin_amdgcn_readfirstlane((par + 2 * (7 * rb - 1)) * (IMG * 192));
    const unsigned mx = ldsb + WBYTES + wid * 1728;
    unsigned slice = idx * 4 + wid; slice -= (slice >= 84) ? 84u : 0u; slice -= (slice >= 42) ? 42u : 0u; slice -= (slice >= 21) ? 21u : 0u;
    const unsigned tw = (slice * 64 + lane) * 128;
    const unsigned mt = ldsb + WBYTES + 9 * XPITCH + wid * 256;
    asm volatile(
        "s_mov_b32 m0, %[mt]\n\t"
        "s_nop 0\n\t"
        "buffer_load_dword %[tw], %[wr], 0 offen lds\n\t"
        "s_add_u32 m0, %[mx], 0\n\t"
        "s_add_u32 s60, %[xb], 0\n\t"
        "buffer_load_dwordx4 %[xo0], %[xr], s60 offen lds\n\t"
        "s_add_u32 m0, %[mx], 1024\n\t"
        "s_mov_b32 exec_hi, 0xfff\n\t"
        "buffer_load_dwordx4 %[xo1], %[xr], s60 offen lds\n\t"
        "s_mov_b32 exec_hi, -1\n\t"
        "s_add_u32 m0, %[mx], 6928\n\t"
        "s_add_u32 s60, %[xb], 86016\n\t"
        "buffer_load_dwordx4 %[xo0], %[xr], s60 offen lds\n\t"
        "s_add_u32 m0, %[mx], 7952\n\t"
        "s_mov_b32 exec_hi, 0xfff\n\t"
        "buffer_load_dwordx4 %[xo1], %[xr], s60 offen lds\n\t"
        "s_mov_b32 exec_hi, -1\n\t"
        "s_add_u32 m0, %[mx], 13856\n\t"
        "s_add_u32 s60, %[xb], 172032\n\t"
        "buffer_load_dwordx4 %[xo0], %[xr], s60 offen lds\n\t"
        "s_add_u32 m0, %[mx], 14880\n\t"
        "s_mov_b32 exec_hi, 0xfff\n\t"
        "buffer_load_dwordx4 %[xo1], %[xr], s60 offen lds\n\t"
        "s_mov_b32 exec_hi, -1\n\t"
        "s_add_u32 m0, %[mx], 20784\n\t"
        "s_add_u32 s60, %[xb], 258048\n\t"
        "buffer_load_dwordx4 %[xo0], %[xr], s60 offen lds\n\t"
        "s_add_u32 m0, %[mx], 21808\n\t"
        "s_mov_b32 exec_hi, 0xfff\n\t"
        "buffer_load_dwordx4 %[xo1], %[xr], s60 offen lds\n\t"
        "s_mov_b32 exec_hi, -1\n\t"
        :
        : [xo0] "v"(xo[0]), [xo1] "v"(xo[1]), [tw] "v"(tw), [xr] "s"(xr), [wr] "s"(wr), [xb] "s"(xb), [mx] "s"(mx), [mt] "s"(mt)
        : "v249", "v250", "s60", "s61", "s62", "s63", "scc", "memory");
    __builtin_amdgcn_sched_barrier(0);

    unsigned wo[5];
#pragma unroll
    for (unsigned u = 0; u < 5; ++u) {
        unsigned n = 288 * wid + 64 * u + lane;
        unsigned cgl = (n * 43691u) >> 19, sc = n - 12 * cgl;
        unsigned src = sc ^ ((cgl >> 2) & 2);
        wo[u] = cgl * 192 + (cgl >> 5) * 49152 + src * 16 - ((u < 4) ? 1024 * u : 0);
    }
    const unsigned mw = ldsb + wid * 4608;
    asm volatile(
        "s_add_u32 m0, %[mw], 0\n\t"
        "s_movk_i32 s61, 0\n\t"
        "buffer_load_dwordx4 %[wo0], %[wr], s61 offen lds\n\t"
        "buffer_load_dwordx4 %[wo1], %[wr], s61 offen offset:1024 lds\n\t"
        "buffer_load_dwordx4 %[wo2], %[wr], s61 offen offset:2048 lds\n\t"
        "buffer_load_dwordx4 %[wo3], %[wr], s61 offen offset:3072 lds\n\t"
        "s_add_u32 m0, %[mw], 4096\n\t"
        "s_mov_b32 exec_hi, 0\n\t"
        "buffer_load_dwordx4 %[wo4], %[wr], s61 offen lds\n\t"
        "s_mov_b32 exec_hi, -1\n\t"
        "s_add_u32 m0, %[mw], 18432\n\t"
        "s_movk_i32 s61, 6144\n\t"
        "buffer_load_dwordx4 %[wo0], %[wr], s61 offen lds\n\t"
        "buffer_load_dwordx4 %[wo1], %[wr], s61 offen offset:1024 lds\n\t"
        "buffer_load_dwordx4 %[wo2], %[wr], s61 offen offset:2048 lds\n\t"
        "buffer_load_dwordx4 %[wo3], %[wr], s61 offen offset:3072 lds\n\t"
        "s_add_u32 m0, %[mw], 22528\n\t"
        "s_mov_b32 exec_hi, 0\n\t"
        "buffer_load_dwordx4 %[wo4], %[wr], s61 offen lds\n\t"
        "s_mov_b32 exec_hi, -1\n\t"
        :
        : [wo0] "v"(wo[0]), [wo1] "v"(wo[1]), [wo2] "v"(wo[2]), [wo3] "v"(wo[3]), [wo4] "v"(wo[4]),
          [xo0] "v"(xo[0]), [xo1] "v"(xo[1]), [xb] "s"(xb), [mx] "s"(mx), [xr] "s"(xr), [wr] "s"(wr), [mw] "s"(mw)
        : "v249", "v250", "s60", "s61", "s62", "s63", "scc", "memory");
    __builtin_amdgcn_sched_barrier(0);

    auto orr = __builtin_amdgcn_make_buffer_rsrc((void*)out, 0, IMG * IMG * 192, 0x00020000);
    const unsigned px = lane & 15, kg = lane >> 4;
    unsigned xa[3];
#pragma unroll
    for (unsigned j = 0; j < 3; ++j) {
        unsigned p = 16 * mh + px + 2 * j;
        xa[j] = ldsb + WBYTES + p * 192 + (kg ^ ((p >> 2) & 3)) * 16 + 8 * (kg & 1);
    }
    const unsigned q4 = (lane & 15) >> 2, p4 = lane & 3;
    unsigned wa[3][2];
#pragma unroll
    for (unsigned t = 0; t < 3; ++t)
#pragma unroll
        for (unsigned tt = 0; tt < 2; ++tt) {
            unsigned c = 8 * kg + 4 * (tt ^ (kg & 1)) + q4;
            unsigned ch = (6 * nh + 2 * t + (p4 >> 1)) ^ ((kg & 1) << 1);
            wa[t][tt] = ldsb + c * 192 + ch * 16 + (p4 & 1) * 8;
        }
    const unsigned pixb = ldsb + (16 * mh + px) * 208 + 96 * nh;
    const unsigned sw1 = pixb + 32 * (kg & 1) + 16 * (kg >> 1);
    const unsigned sw2 = pixb + 64 + 16 * (kg >> 1) + (kg & 1) * 6656;
    const unsigned sw3 = pixb + 64 + 8 * kg;
    unsigned rl[3], go[3];
#pragma unroll
    for (unsigned k = 0; k < 3; ++k) {
        unsigned c = 64 * k + lane, row = (c >= 96) ? 1u : 0u, cc = 96 * wid + c - 96 * row;
        unsigned pxl = (cc * 43691u) >> 19;
        rl[k] = ldsb + row * 6656 + cc * 16 + pxl * 16;
        go[k] = row * 86016 + 32 * cb * 192 + cc * 16;
    }
    const unsigned ob = __builtin_amdgcn_readfirstlane((par + 14 * rb) * (IMG * 192));

    asm volatile(
        "v_xor_b32 v240, 8, %[xa0]\n\t"
        "v_xor_b32 v241, 8, %[xa1]\n\t"
        "v_xor_b32 v242, 8, %[xa2]\n\t"
        "v_add_u32 v243, 55296, %[wa00]\n\t"
        "v_add_u32 v244, 55296, %[wa01]\n\t"
        "v_add_u32 v245, 55296, %[wa10]\n\t"
        "v_add_u32 v246, 55296, %[wa11]\n\t"
        "v_add_u32 v247, 55296, %[wa20]\n\t"
        "v_add_u32 v248, 55296, %[wa21]\n\t"
        "s_waitcnt vmcnt(5)\n\t"
        "s_barrier\n\t"
        "ds_read_b64 v[124:125], %[xa0] offset:0\n\t"
        "ds_read_b64 v[126:127], v240 offset:0\n\t"
        "ds_read_b64 v[128:129], %[xa0] offset:6928\n\t"
        "ds_read_b64 v[130:131], v240 offset:6928\n\t"
        "ds_read_b64 v[132:133], %[xa0] offset:13856\n\t"
        "ds_read_b64 v[134:135], v240 offset:13856\n\t"
        "ds_read_b64 v[136:137], %[xa0] offset:20784\n\t"
        "ds_read_b64 v[138:139], v240 offset:20784\n\t"
        "ds_read_b64_tr_b16 a[0:1], %[wa00] offset:0\n\t"
        "ds_read_b64_tr_b16 a[2:3], %[wa01] offset:0\n\t"
        "s_waitcnt lgkmcnt(0)\n\t"
        "v_mfma_f32_16x16x32_bf16 v[40:43], a[0:3], v[124:127], 0\n\t"
        "ds_read_b64_tr_b16 a[4:5], %[wa10] offset:0\n\t"
        "ds_read_b64_tr_b16 a[6:7], %[wa11] offset:0\n\t"
        "s_waitcnt lgkmcnt(0)\n\t"
        "v_mfma_f32_16x16x32_bf16 v[44:47], a[4:7], v[124:127], 0\n\t"
        "s_add_u32 m0, %[mw], 36864\n\t"
        "s_movk_i32 s61, 12288\n\t"
        "buffer_load_dwordx4 %[wo0], %[wr], s61 offen lds\n\t"
        "ds_read_b64_tr_b16 a[8:9], %[wa20] offset:0\n\t"
        "ds_read_b64_tr_b16 a[10:11], %[wa21] offset:0\n\t"
        "s_waitcnt lgkmcnt(0)\n\t"
        "v_mfma_f32_16x16x32_bf16 v[48:51], a[8:11], v[124:127], 0\n\t"
        "ds_read_b64_tr_b16 a[12:13], %[wa00] offset:6144\n\t"
        "ds_read_b64_tr_b16 a[14:15], %[wa01] offset:6144\n\t"
        "v_mfma_f32_16x16x32_bf16 v[52:55], a[0:3], v[128:131], 0\n\t"
        "buffer_load_dwordx4 %[wo1], %[wr], s61 offen offset:1024 lds\n\t"
        "ds_read_b64 v[140:141], %[xa0] offset:64\n\t"
        "ds_read_b64 v[142:143], v240 offset:64\n\t"
        "v_mfma_f32_16x16x32_bf16 v[56:59], a[4:7], v[128:131], 0\n\t"
        "ds_read_b64_tr_b16 a[16:17], %[wa10] offset:6144\n\t"
        "ds_read_b64_tr_b16 a[18:19], %[wa11] offset:6144\n\t"
        "v_mfma_f32_16x16x32_bf16 v[60:63], a[8:11], v[128:131], 0\n\t"
        "buffer_load_dwordx4 %[wo2], %[wr], s61 offen offset:2048 lds\n\t"
        "ds_read_b64_tr_b16 a[20:21], %[wa20] offset:6144\n\t"
        "s_waitcnt lgkmcnt(5)\n\t"
        "v_mfma_f32_16x16x32_bf16 v[40:43], a[12:15], v[128:131], v[40:43]\n\t"
        "ds_read_b64_tr_b16 a[22:23], %[wa21] offset:6144\n\t"
        "ds_read_b64_tr_b16 a[24:25], %[wa00] offset:12288\n\t"
        "s_waitcnt lgkmcnt(3)\n\t"
        "v_mfma_f32_16x16x32_bf16 v[44:47], a[16:19], v[128:131], v[44:47]\n\t"
        "buffer_load_dwordx4 %[wo3], %[wr], s61 offen offset:3072 lds\n\t"
        "ds_read_b64_tr_b16 a[26:27], %[wa01] offset:12288\n\t"
        "s_waitcnt lgkmcnt(2)\n\t"
        "v_mfma_f32_16x16x32_bf16 v[48:51], a[20:23], v[128:131], v[48:51]\n\t"
        "ds_read_b64_tr_b16 a[28:29], %[wa10] offset:12288\n\t"
        "ds_read_b64_tr_b16 a[30:31], %[wa11] offset:12288\n\t"
        "ds_read_b64 v[144:145], %[xa0] offset:6992\n\t"
        "ds_read_b64 v[146:147], v240 offset:6992\n\t"
        "ds_read_b64_tr_b16 a[32:33], %[wa20] offset:12288\n\t"
        "ds_read_b64_tr_b16 a[34:35], %[wa21] offset:12288\n\t"
        "s_waitcnt vmcnt(4)\n\t"
        "s_waitcnt lgkmcnt(0)\n\t"
        "s_barrier\n\t"
        "v_mfma_f32_16x16x32_bf16 v[52:55], a[12:15], v[132:135], v[52:55]\n\t"
        "s_add_u32 m0, %[mw], 40960\n\t"
        "s_mov_b32 exec_hi, 0\n\t"
        "buffer_load_dwordx4 %[wo4], %[wr], s61 offen lds\n\t"
        "s_mov_b32 exec_hi, -1\n\t"
        "ds_read_b64_tr_b16 a[36:37], %[wa00] offset:18432\n\t"
        "v_mfma_f32_16x16x32_bf16 v[56:59], a[16:19], v[132:135], v[56:59]\n\t"
        "ds_read_b64_tr_b16 a[38:39], %[wa01] offset:18432\n\t"
        "ds_read_b64_tr_b16 a[40:41], %[wa10] offset:18432\n\t"
        "v_mfma_f32_16x16x32_bf16 v[60:63], a[20:23], v[132:135], v[60:63]\n\t"
        "s_add_u32 m0, %[mw], 55296\n\t"
        "s_movk_i32 s61, 18432\n\t"
        "buffer_load_dwordx4 %[wo0], %[wr], s61 offen lds\n\t"
        "ds_read_b64_tr_b16 a[42:43], %[wa11] offset:18432\n\t"
        "v_mfma_f32_16x16x32_bf16 v[40:43], a[24:27], v[132:135], v[40:43]\n\t"
        "ds_read_b64_tr_b16 a[44:45], %[wa20] offset:18432\n\t"
        "ds_read_b64_tr_b16 a[46:47], %[wa21] offset:18432\n\t"
        "v_mfma_f32_16x16x32_bf16 v[44:47], a[28:31], v[132:135], v[44:47]\n\t"
        "buffer_load_dwordx4 %[wo1], %[wr], s61 offen offset:1024 lds\n\t"
        "ds_read_b64_tr_b16 a[48:49], %[wa00] offset:24576\n\t"
        "v_mfma_f32_16x16x32_bf16 v[48:51], a[32:35], v[132:135], v[48:51]\n\t"
        "ds_read_b64_tr_b16 a[50:51], %[wa01] offset:24576\n\t"
        "ds_read_b64_tr_b16 a[52:53], %[wa10] offset:24576\n\t"
        "v_mfma_f32_16x16x32_bf16 v[52:55], a[24:27], v[136:139], v[52:55]\n\t"
        "buffer_load_dwordx4 %[wo2], %[wr], s61 offen offset:2048 lds\n\t"
        "ds_read_b64 v[124:125], %[xa0] offset:13920\n\t"
        "ds_read_b64 v[126:127], v240 offset:13920\n\t"
        "v_mfma_f32_16x16x32_bf16 v[56:59], a[28:31], v[136:139], v[56:59]\n\t"
        "ds_read_b64_tr_b16 a[54:55], %[wa11] offset:24576\n\t"
        "ds_read_b64_tr_b16 a[56:57], %[wa20] offset:24576\n\t"
        "v_mfma_f32_16x16x32_bf16 v[60:63], a[32:35], v[136:139], v[60:63]\n\t"
        "buffer_load_dwordx4 %[wo3], %[wr], s61 offen offset:3072 lds\n\t"
        "ds_read_b64_tr_b16 a[58:59], %[wa21] offset:24576\n\t"
        "s_waitcnt lgkmcnt(12)\n\t"
        "v_mfma_f32_16x16x32_bf16 v[40:43], a[36:39], v[140:143], v[40:43]\n\t"
        "ds_read_b64 v[128:129], %[xa0] offset:20848\n\t"
        "ds_read_b64 v[130:131], v240 offset:20848\n\t"
        "ds_read_b64_tr_b16 a[60:61], %[wa00] offset:30720\n\t"
        "s_waitcnt lgkmcnt(13)\n\t"
        "v_mfma_f32_16x16x32_bf16 v[44:47], a[40:43], v[140:143], v[44:47]\n\t"
        "s_add_u32 m0, %[mw], 59392\n\t"
        "s_mov_b32 exec_hi, 0\n\t"
        "buffer_load_dwordx4 %[wo4], %[wr], s61 offen lds\n\t"
        "s_mov_b32 exec_hi, -1\n\t"
        "ds_read_b64_tr_b16 a[62:63], %[wa01] offset:30720\n\t"
        "s_waitcnt lgkmcnt(12)\n\t"
        "v_mfma_f32_16x16x32_bf16 v[48:51], a[44:47], v[140:143], v[48:51]\n\t"
        "ds_read_b64_tr_b16 a[64:65], %[wa10] offset:30720\n\t"
        "ds_read_b64_tr_b16 a[66:67], %[wa11] offset:30720\n\t"
        "v_mfma_f32_16x16x32_bf16 v[52:55], a[36:39], v[144:147], v[52:55]\n\t"
        "s_add_u32 m0, %[mw], 73728\n\t"
        "s_movk_i32 s61, 24576\n\t"
        "buffer_load_dwordx4 %[wo0], %[wr], s61 offen lds\n\t"
        "ds_read_b64 v[132:133], %[xa0] offset:128\n\t"
        "ds_read_b64 v[134:135], v240 offset:128\n\t"
        "v_mfma_f32_16x16x32_bf16 v[56:59], a[40:43], v[144:147], v[56:59]\n\t"
        "ds_read_b64_tr_b16 a[68:69], %[wa20] offset:30720\n\t"
        "ds_read_b64_tr_b16 a[70:71], %[wa21] offset:30720\n\t"
        "v_mfma_f32_16x16x32_bf16 v[60:63], a[44:47], v[144:147], v[60:63]\n\t"
        "buffer_load_dwordx4 %[wo1], %[wr], s61 offen offset:1024 lds\n\t"
        "s_waitcnt lgkmcnt(15)\n\t"
        "v_mfma_f32_16x16x32_bf16 v[40:43], a[48:51], v[144:147], v[40:43]\n\t"
        "s_waitcnt lgkmcnt(12)\n\t"
        "v_mfma_f32_16x16x32_bf16 v[44:47], a[52:55], v[144:147], v[44:47]\n\t"
        "buffer_load_dwordx4 %[wo2], %[wr], s61 offen offset:2048 lds\n\t"
        "s_waitcnt lgkmcnt(10)\n\t"
        "v_mfma_f32_16x16x32_bf16 v[48:51], a[56:59], v[144:147], v[48:51]\n\t"
        "ds_read_b64 v[136:137], %[xa0] offset:7056\n\t"
        "ds_read_b64 v[138:139], v240 offset:7056\n\t"
        "s_waitcnt vmcnt(8)\n\t"
        "s_waitcnt lgkmcnt(2)\n\t"
        "s_barrier\n\t"
        "v_mfma_f32_16x16x32_bf16 v[52:55], a[48:51], v[124:127], v[52:55]\n\t"
        "buffer_load_dwordx4 %[wo3], %[wr], s61 offen offset:3072 lds\n\t"
        "ds_read_b64_tr_b16 a[72:73], %[wa00] offset:36864\n\t"
        "v_mfma_f32_16x16x32_bf16 v[56:59], a[52:55], v[124:127], v[56:59]\n\t"
        "ds_read_b64_tr_b16 a[74:75], %[wa01] offset:36864\n\t"
        "ds_read_b64_tr_b16 a[76:77], %[wa10] offset:36864\n\t"
        "v_mfma_f32_16x16x32_bf16 v[60:63], a[56:59], v[124:127], v[60:63]\n\t"
        "s_add_u32 m0, %[mw], 77824\n\t"
        "s_mov_b32 exec_hi, 0\n\t"
        "buffer_load_dwordx4 %[wo4], %[wr], s61 offen lds\n\t"
        "s_mov_b32 exec_hi, -1\n\t"
        "ds_read_b64_tr_b16 a[78:79], %[wa11] offset:36864\n\t"
        "v_mfma_f32_16x16x32_bf16 v[40:43], a[60:63], v[124:127], v[40:43]\n\t"
        "ds_read_b64_tr_b16 a[80:81], %[wa20] offset:36864\n\t"
        "ds_read_b64_tr_b16 a[82:83], %[wa21] offset:36864\n\t"
        "v_mfma_f32_16x16x32_bf16 v[44:47], a[64:67], v[124:127], v[44:47]\n\t"
        "s_add_u32 m0, %[mw], 0\n\t"
        "s_movk_i32 s61, 30720\n\t"
        "buffer_load_dwordx4 %[wo0], %[wr], s61 offen lds\n\t"
        "ds_read_b64_tr_b16 a[84:85], %[wa00] offset:43008\n\t"
        "v_mfma_f32_16x16x32_bf16 v[48:51], a[68:71], v[124:127], v[48:51]\n\t"
        "ds_read_b64_tr_b16 a[86:87], %[wa01] offset:43008\n\t"
        "ds_read_b64_tr_b16 a[88:89], %[wa10] offset:43008\n\t"
        "v_mfma_f32_16x16x32_bf16 v[52:55], a[60:63], v[128:131], v[52:55]\n\t"
        "buffer_load_dwordx4 %[wo1], %[wr], s61 offen offset:1024 lds\n\t"
        "ds_read_b64 v[140:141], %[xa0] offset:13984\n\t"
        "ds_read_b64 v[142:143], v240 offset:13984\n\t"
        "v_mfma_f32_16x16x32_bf16 v[56:59], a[64:67], v[128:131], v[56:59]\n\t"
        "ds_read_b64_tr_b16 a[90:91], %[wa11] offset:43008\n\t"
        "ds_read_b64_tr_b16 a[92:93], %[wa20] offset:43008\n\t"
        "v_mfma_f32_16x16x32_bf16 v[60:63], a[68:71], v[128:131], v[60:63]\n\t"
        "buffer_load_dwordx4 %[wo2], %[wr], s61 offen offset:2048 lds\n\t"
        "ds_read_b64_tr_b16 a[94:95], %[wa21] offset:43008\n\t"
        "s_waitcnt lgkmcnt(12)\n\t"
        "v_mfma_f32_16x16x32_bf16 v[40:43], a[72:75], v[132:135], v[40:43]\n\t"
        "ds_read_b64 v[144:145], %[xa0] offset:20912\n\t"
        "ds_read_b64 v[146:147], v240 offset:20912\n\t"
        "ds_read_b64_tr_b16 a[96:97], %[wa00] offset:49152\n\t"
        "s_waitcnt lgkmcnt(13)\n\t"
        "v_mfma_f32_16x16x32_bf16 v[44:47], a[76:79], v[132:135], v[44:47]\n\t"
        "buffer_load_dwordx4 %[wo3], %[wr], s61 offen offset:3072 lds\n\t"
        "ds_read_b64_tr_b16 a[98:99], %[wa01] offset:49152\n\t"
        "s_waitcnt lgkmcnt(12)\n\t"
        "v_mfma_f32_16x16x32_bf16 v[48:51], a[80:83], v[132:135], v[48:51]\n\t"
        "ds_read_b64_tr_b16 a[100:101], %[wa10] offset:49152\n\t"
        "ds_read_b64_tr_b16 a[102:103], %[wa11] offset:49152\n\t"
        "v_mfma_f32_16x16x32_bf16 v[52:55], a[72:75], v[136:139], v[52:55]\n\t"
        "s_add_u32 m0, %[mw], 4096\n\t"
        "s_mov_b32 exec_hi, 0\n\t"
        "buffer_load_dwordx4 %[wo4], %[wr], s61 offen lds\n\t"
        "s_mov_b32 exec_hi, -1\n\t"
        "ds_read_b64 v[124:125], %[xa1] offset:0\n\t"
        "ds_read_b64 v[126:127], v241 offset:0\n\t"
        "v_mfma_f32_16x16x32_bf16 v[56:59], a[76:79], v[136:139], v[56:59]\n\t"
        "ds_read_b64_tr_b16 a[104:105], %[wa20] offset:49152\n\t"
        "ds_read_b64_tr_b16 a[106:107], %[wa21] offset:49152\n\t"
        "v_mfma_f32_16x16x32_bf16 v[60:63], a[80:83], v[136:139], v[60:63]\n\t"
        "s_add_u32 m0, %[mw], 18432\n\t"
        "s_mov_b32 s61, 36864\n\t"
        "buffer_load_dwordx4 %[wo0], %[wr], s61 offen lds\n\t"
        "s_waitcnt lgkmcnt(15)\n\t"
        "v_mfma_f32_16x16x32_bf16 v[40:43], a[84:87], v[136:139], v[40:43]\n\t"
        "s_waitcnt lgkmcnt(12)\n\t"
        "v_mfma_f32_16x16x32_bf16 v[44:47], a[88:91], v[136:139], v[44:47]\n\t"
        "buffer_load_dwordx4 %[wo1], %[wr], s61 offen offset:1024 lds\n\t"
        "s_waitcnt lgkmcnt(10)\n\t"
        "v_mfma_f32_16x16x32_bf16 v[48:51], a[92:95], v[136:139], v[48:51]\n\t"
        "ds_read_b64 v[128:129], %[xa1] offset:6928\n\t"
        "ds_read_b64 v[130:131], v241 offset:6928\n\t"
        "s_waitcnt vmcnt(12)\n\t"
        "s_waitcnt lgkmcnt(2)\n\t"
        "s_barrier\n\t"
        "v_mfma_f32_16x16x32_bf16 v[52:55], a[84:87], v[140:143], v[52:55]\n\t"
        "buffer_load_dwordx4 %[wo2], %[wr], s61 offen offset:2048 lds\n\t"
        "ds_read_b64_tr_b16 a[108:109], v243 offset:0\n\t"
        "v_mfma_f32_16x16x32_bf16 v[56:59], a[88:91], v[140:143], v[56:59]\n\t"
        "ds_read_b64_tr_b16 a[110:111], v244 offset:0\n\t"
        "ds_read_b64_tr_b16 a[112:113], v245 offset:0\n\t"
        "v_mfma_f32_16x16x32_bf16 v[60:63], a[92:95], v[140:143], v[60:63]\n\t"
        "buffer_load_dwordx4 %[wo3], %[wr], s61 offen offset:3072 lds\n\t"
        "ds_read_b64_tr_b16 a[114:115], v246 offset:0\n\t"
        "v_mfma_f32_16x16x32_bf16 v[40:43], a[96:99], v[140:143], v[40:43]\n\t"
        "ds_read_b64_tr_b16 a[116:117], v247 offset:0\n\t"
        "ds_read_b64_tr_b16 a[118:119], v248 offset:0\n\t"
        "v_mfma_f32_16x16x32_bf16 v[44:47], a[100:103], v[140:143], v[44:47]\n\t"
        "s_add_u32 m0, %[mw], 22528\n\t"
        "s_mov_b32 exec_hi, 0\n\t"
        "buffer_load_dwordx4 %[wo4], %[wr], s61 offen lds\n\t"
        "s_mov_b32 exec_hi, -1\n\t"
        "ds_read_b64_tr_b16 a[120:121], v243 offset:6144\n\t"
        "v_mfma_f32_16x16x32_bf16 v[48:51], a[104:107], v[140:143], v[48:51]\n\t"
        "ds_read_b64_tr_b16 a[122:123], v244 offset:6144\n\t"
        "ds_read_b64_tr_b16 a[124:125], v245 offset:6144\n\t"
        "v_mfma_f32_16x16x32_bf16 v[52:55], a[96:99], v[144:147], v[52:55]\n\t"
        "s_add_u32 m0, %[mw], 36864\n\t"
        "s_mov_b32 s61, 43008\n\t"
        "buffer_load_dwordx4 %[wo0], %[wr], s61 offen lds\n\t"
        "ds_read_b64 v[132:133], %[xa1] offset:13856\n\t"
        "ds_read_b64 v[134:135], v241 offset:13856\n\t"
        "v_mfma_f32_16x16x32_bf16 v[56:59], a[100:103], v[144:147], v[56:59]\n\t"
        "ds_read_b64_tr_b16 a[126:127], v246 offset:6144\n\t"
        "ds_read_b64_tr_b16 a[128:129], v247 offset:6144\n\t"
        "v_mfma_f32_16x16x32_bf16 v[60:63], a[104:107], v[144:147], v[60:63]\n\t"
        "buffer_load_dwordx4 %[wo1], %[wr], s61 offen offset:1024 lds\n\t"
        "ds_read_b64_tr_b16 a[130:131], v248 offset:6144\n\t"
        "s_waitcnt lgkmcnt(12)\n\t"
        "v_mfma_f32_16x16x32_bf16 v[40:43], a[108:111], v[124:127], v[40:43]\n\t"
        "ds_read_b64 v[136:137], %[xa1] offset:20784\n\t"
        "ds_read_b64 v[138:139], v241 offset:20784\n\t"
        "ds_read_b64_tr_b16 a[132:133], v243 offset:12288\n\t"
        "s_waitcnt lgkmcnt(13)\n\t"
        "v_mfma_f32_16x16x32_bf16 v[44:47], a[112:115], v[124:127], v[44:47]\n\t"
        "buffer_load_dwordx4 %[wo2], %[wr], s61 offen offset:2048 lds\n\t"
        "ds_read_b64_tr_b16 a[134:135], v244 offset:12288\n\t"
        "s_waitcnt lgkmcnt(12)\n\t"
        "v_mfma_f32_16x16x32_bf16 v[48:51], a[116:119], v[124:127], v[48:51]\n\t"
        "ds_read_b64_tr_b16 a[136:137], v245 offset:12288\n\t"
        "ds_read_b64_tr_b16 a[138:139], v246 offset:12288\n\t"
        "v_mfma_f32_16x16x32_bf16 v[52:55], a[108:111], v[128:131], v[52:55]\n\t"
        "buffer_load_dwordx4 %[wo3], %[wr], s61 offen offset:3072 lds\n\t"
        "ds_read_b64 v[140:141], %[xa1] offset:64\n\t"
        "ds_read_b64 v[142:143], v241 offset:64\n\t"
        "v_mfma_f32_16x16x32_bf16 v[56:59], a[112:115], v[128:131], v[56:59]\n\t"
        "ds_read_b64_tr_b16 a[140:141], v247 offset:12288\n\t"
        "ds_read_b64_tr_b16 a[142:143], v248 offset:12288\n\t"
        "v_mfma_f32_16x16x32_bf16 v[60:63], a[116:119], v[128:131], v[60:63]\n\t"
        "s_add_u32 m0, %[mw], 40960\n\t"
        "s_mov_b32 exec_hi, 0\n\t"
        "buffer_load_dwordx4 %[wo4], %[wr], s61 offen lds\n\t"
        "s_mov_b32 exec_hi, -1\n\t"
        "s_waitcnt lgkmcnt(15)\n\t"
        "v_mfma_f32_16x16x32_bf16 v[40:43], a[120:123], v[128:131], v[40:43]\n\t"
        "s_waitcnt lgkmcnt(12)\n\t"
        "v_mfma_f32_16x16x32_bf16 v[44:47], a[124:127], v[128:131], v[44:47]\n\t"
        "s_waitcnt lgkmcnt(10)\n\t"
        "v_mfma_f32_16x16x32_bf16 v[48:51], a[128:131], v[128:131], v[48:51]\n\t"
        "ds_read_b64 v[144:145], %[xa1] offset:6992\n\t"
        "ds_read_b64 v[146:147], v241 offset:6992\n\t"
        "s_waitcnt vmcnt(15)\n\t"
        "s_waitcnt lgkmcnt(2)\n\t"
        "s_barrier\n\t"
        "v_mfma_f32_16x16x32_bf16 v[52:55], a[120:123], v[132:135], v[52:55]\n\t"
        "s_add_u32 m0, %[mw], 55296\n\t"
        "s_mov_b32 s61, 49152\n\t"
        "buffer_load_dwordx4 %[wo0], %[wr], s61 offen lds\n\t"
        "ds_read_b64_tr_b16 a[144:145], v243 offset:18432\n\t"
        "v_mfma_f32_16x16x32_bf16 v[56:59], a[124:127], v[132:135], v[56:59]\n\t"
        "ds_read_b64_tr_b16 a[146:147], v244 offset:18432\n\t"
        "ds_read_b64_tr_b16 a[148:149], v245 offset:18432\n\t"
        "v_mfma_f32_16x16x32_bf16 v[60:63], a[128:131], v[132:135], v[60:63]\n\t"
        "buffer_load_dwordx4 %[wo1], %[wr], s61 offen offset:1024 lds\n\t"
        "ds_read_b64_tr_b16 a[150:151], v246 offset:18432\n\t"
        "v_mfma_f32_16x16x32_bf16 v[40:43], a[132:135], v[132:135], v[40:43]\n\t"
        "ds_read_b64_tr_b16 a[152:153], v247 offset:18432\n\t"
        "ds_read_b64_tr_b16 a[154:155], v248 offset:18432\n\t"
        "v_mfma_f32_16x16x32_bf16 v[44:47], a[136:139], v[132:135], v[44:47]\n\t"
        "buffer_load_dwordx4 %[wo2], %[wr], s61 offen offset:2048 lds\n\t"
        "ds_read_b64_tr_b16 a[156:157], v243 offset:24576\n\t"
        "v_mfma_f32_16x16x32_bf16 v[48:51], a[140:143], v[132:135], v[48:51]\n\t"
        "ds_read_b64_tr_b16 a[158:159], v244 offset:24576\n\t"
        "ds_read_b64_tr_b16 a[160:161], v245 offset:24576\n\t"
        "v_mfma_f32_16x16x32_bf16 v[52:55], a[132:135], v[136:139], v[52:55]\n\t"
        "buffer_load_dwordx4 %[wo3], %[wr], s61 offen offset:3072 lds\n\t"
        "ds_read_b64 v[124:125], %[xa1] offset:13920\n\t"
        "ds_read_b64 v[126:127], v241 offset:13920\n\t"
        "v_mfma_f32_16x16x32_bf16 v[56:59], a[136:139], v[136:139], v[56:59]\n\t"
        "ds_read_b64_tr_b16 a[162:163], v246 offset:24576\n\t"
        "ds_read_b64_tr_b16 a[164:165], v247 offset:24576\n\t"
        "v_mfma_f32_16x16x32_bf16 v[60:63], a[140:143], v[136:139], v[60:63]\n\t"
        "s_add_u32 m0, %[mw], 59392\n\t"
        "s_mov_b32 exec_hi, 0\n\t"
        "buffer_load_dwordx4 %[wo4], %[wr], s61 offen lds\n\t"
        "s_mov_b32 exec_hi, -1\n\t"
        "ds_read_b64_tr_b16 a[166:167], v248 offset:24576\n\t"
        "s_waitcnt lgkmcnt(12)\n\t"
        "v_mfma_f32_16x16x32_bf16 v[40:43], a[144:147], v[140:143], v[40:43]\n\t"
        "ds_read_b64 v[128:129], %[xa1] offset:20848\n\t"
        "ds_read_b64 v[130:131], v241 offset:20848\n\t"
        "ds_read_b64_tr_b16 a[168:169], v243 offset:30720\n\t"
        "s_waitcnt lgkmcnt(13)\n\t"
        "v_mfma_f32_16x16x32_bf16 v[44:47], a[148:151], v[140:143], v[44:47]\n\t"
        "s_add_u32 m0, %[mx], 27712\n\t"
        "s_add_u32 s60, %[xb], 344064\n\t"
        "buffer_load_dwordx4 %[xo0], %[xr], s60 offen lds\n\t"
        "ds_read_b64_tr_b16 a[170:171], v244 offset:30720\n\t"
        "s_waitcnt lgkmcnt(12)\n\t"
        "v_mfma_f32_16x16x32_bf16 v[48:51], a[152:155], v[140:143], v[48:51]\n\t"
        "ds_read_b64_tr_b16 a[172:173], v245 offset:30720\n\t"
        "ds_read_b64_tr_b16 a[174:175], v246 offset:30720\n\t"
        "v_mfma_f32_16x16x32_bf16 v[52:55], a[144:147], v[144:147], v[52:55]\n\t"
        "s_add_u32 m0, %[mx], 28736\n\t"
        "s_mov_b32 exec_hi, 0xfff\n\t"
        "buffer_load_dwordx4 %[xo1], %[xr], s60 offen lds\n\t"
        "s_mov_b32 exec_hi, -1\n\t"
        "ds_read_b64 v[132:133], %[xa1] offset:128\n\t"
        "ds_read_b64 v[134:135], v241 offset:128\n\t"
        "v_mfma_f32_16x16x32_bf16 v[56:59], a[148:151], v[144:147], v[56:59]\n\t"
        "ds_read_b64_tr_b16 a[176:177], v247 offset:30720\n\t"
        "ds_read_b64_tr_b16 a[178:179], v248 offset:30720\n\t"
        "v_mfma_f32_16x16x32_bf16 v[60:63], a[152:155], v[144:147], v[60:63]\n\t"
        "s_add_u32 m0, %[mx], 34640\n\t"
        "s_add_u32 s60, %[xb], 430080\n\t"
        "buffer_load_dwordx4 %[xo0], %[xr], s60 offen lds\n\t"
        "s_waitcnt lgkmcnt(15)\n\t"
        "v_mfma_f32_16x16x32_bf16 v[40:43], a[156:159], v[144:147], v[40:43]\n\t"
        "s_waitcnt lgkmcnt(12)\n\t"
        "v_mfma_f32_16x16x32_bf16 v[44:47], a[160:163], v[144:147], v[44:47]\n\t"
        "s_add_u32 m0, %[mx], 35664\n\t"
        "s_mov_b32 exec_hi, 0xfff\n\t"
        "buffer_load_dwordx4 %[xo1], %[xr], s60 offen lds\n\t"
        "s_mov_b32 exec_hi, -1\n\t"
        "s_waitcnt lgkmcnt(10)\n\t"
        "v_mfma_f32_16x16x32_bf16 v[48:51], a[164:167], v[144:147], v[48:51]\n\t"
        "ds_read_b64 v[136:137], %[xa1] offset:7056\n\t"
        "ds_read_b64 v[138:139], v241 offset:7056\n\t"
        "s_waitcnt vmcnt(19)\n\t"
        "s_waitcnt lgkmcnt(2)\n\t"
        "s_barrier\n\t"
        "v_mfma_f32_16x16x32_bf16 v[52:55], a[156:159], v[124:127], v[52:55]\n\t"
        "s_add_u32 m0, %[mx], 41568\n\t"
        "s_add_u32 s60, %[xb], 516096\n\t"
        "buffer_load_dwordx4 %[xo0], %[xr], s60 offen lds\n\t"
        "ds_read_b64_tr_b16 a[180:181], %[wa00] offset:0\n\t"
        "v_mfma_f32_16x16x32_bf16 v[56:59], a[160:163], v[124:127], v[56:59]\n\t"
        "ds_read_b64_tr_b16 a[182:183], %[wa01] offset:0\n\t"
        "ds_read_b64_tr_b16 a[184:185], %[wa10] offset:0\n\t"
        "v_mfma_f32_16x16x32_bf16 v[60:63], a[164:167], v[124:127], v[60:63]\n\t"
        "s_add_u32 m0, %[mx], 42592\n\t"
        "s_mov_b32 exec_hi, 0xfff\n\t"
        "buffer_load_dwordx4 %[xo1], %[xr], s60 offen lds\n\t"
        "s_mov_b32 exec_hi, -1\n\t"
        "ds_read_b64_tr_b16 a[186:187], %[wa11] offset:0\n\t"
        "v_mfma_f32_16x16x32_bf16 v[40:43], a[168:171], v[124:127], v[40:43]\n\t"
        "ds_read_b64_tr_b16 a[188:189], %[wa20] offset:0\n\t"
        "ds_read_b64_tr_b16 a[190:191], %[wa21] offset:0\n\t"
        "v_mfma_f32_16x16x32_bf16 v[44:47], a[172:175], v[124:127], v[44:47]\n\t"
        "s_add_u32 m0, %[mx], 48496\n\t"
        "s_add_u32 s60, %[xb], 602112\n\t"
        "buffer_load_dwordx4 %[xo0], %[xr], s60 offen lds\n\t"
        "ds_read_b64_tr_b16 a[192:193], %[wa00] offset:6144\n\t"
        "v_mfma_f32_16x16x32_bf16 v[48:51], a[176:179], v[124:127], v[48:51]\n\t"
        "ds_read_b64_tr_b16 a[194:195], %[wa01] offset:6144\n\t"
        "ds_read_b64_tr_b16 a[196:197], %[wa10] offset:6144\n\t"
        "v_mfma_f32_16x16x32_bf16 v[52:55], a[168:171], v[128:131], v[52:55]\n\t"
        "s_add_u32 m0, %[mx], 49520\n\t"
        "s_mov_b32 exec_hi, 0xfff\n\t"
        "buffer_load_dwordx4 %[xo1], %[xr], s60 offen lds\n\t"
        "s_mov_b32 exec_hi, -1\n\t"
        "ds_read_b64 v[140:141], %[xa1] offset:13984\n\t"
        "ds_read_b64 v[142:143], v241 offset:13984\n\t"
        "v_mfma_f32_16x16x32_bf16 v[56:59], a[172:175], v[128:131], v[56:59]\n\t"
        "ds_read_b64_tr_b16 a[198:199], %[wa11] offset:6144\n\t"
        "ds_read_b64_tr_b16 a[200:201], %[wa20] offset:6144\n\t"
        "v_mfma_f32_16x16x32_bf16 v[60:63], a[176:179], v[128:131], v[60:63]\n\t"
        "s_add_u32 m0, %[mx], 55424\n\t"
        "s_add_u32 s60, %[xb], 688128\n\t"
        "buffer_load_dwordx4 %[xo0], %[xr], s60 offen lds\n\t"
        "ds_read_b64_tr_b16 a[202:203], %[wa21] offset:6144\n\t"
        "s_waitcnt lgkmcnt(12)\n\t"
        "v_mfma_f32_16x16x32_bf16 v[40:43], a[180:183], v[132:135], v[40:43]\n\t"
        "ds_read_b64 v[144:145], %[xa1] offset:20912\n\t"
        "ds_read_b64 v[146:147], v241 offset:20912\n\t"
        "ds_read_b64_tr_b16 a[204:205], %[wa00] offset:12288\n\t"
        "s_waitcnt lgkmcnt(13)\n\t"
        "v_mfma_f32_16x16x32_bf16 v[44:47], a[184:187], v[132:135], v[44:47]\n\t"
        "ds_read_b64_tr_b16 a[206:207], %[wa01] offset:12288\n\t"
        "ds_read_b64_tr_b16 a[208:209], %[wa10] offset:12288\n\t"
        "s_waitcnt lgkmcnt(13)\n\t"
        "v_mfma_f32_16x16x32_bf16 v[48:51], a[188:191], v[132:135], v[48:51]\n\t"
        "ds_read_b64_tr_b16 a[210:211], %[wa11] offset:12288\n\t"
        "ds_read_b64_tr_b16 a[212:213], %[wa20] offset:12288\n\t"
        "v_mfma_f32_16x16x32_bf16 v[52:55], a[180:183], v[136:139], v[52:55]\n\t"
        "ds_read_b64 v[124:125], %[xa2] offset:0\n\t"
        "ds_read_b64 v[126:127], v242 offset:0\n\t"
        "ds_read_b64_tr_b16 a[214:215], %[wa21] offset:12288\n\t"
        "v_mfma_f32_16x16x32_bf16 v[56:59], a[184:187], v[136:139], v[56:59]\n\t"
        "v_mfma_f32_16x16x32_bf16 v[60:63], a[188:191], v[136:139], v[60:63]\n\t"
        "s_waitcnt lgkmcnt(15)\n\t"
        "v_mfma_f32_16x16x32_bf16 v[40:43], a[192:195], v[136:139], v[40:43]\n\t"
        "s_waitcnt lgkmcnt(12)\n\t"
        "v_mfma_f32_16x16x32_bf16 v[44:47], a[196:199], v[136:139], v[44:47]\n\t"
        "s_waitcnt lgkmcnt(10)\n\t"
        "v_mfma_f32_16x16x32_bf16 v[48:51], a[200:203], v[136:139], v[48:51]\n\t"
        "ds_read_b64 v[128:129], %[xa2] offset:6928\n\t"
        "ds_read_b64 v[130:131], v242 offset:6928\n\t"
        "s_waitcnt vmcnt(19)\n\t"
        "s_waitcnt lgkmcnt(2)\n\t"
        "s_barrier\n\t"
        "v_mfma_f32_16x16x32_bf16 v[52:55], a[192:195], v[140:143], v[52:55]\n\t"
        "s_add_u32 m0, %[mx], 56448\n\t"
        "s_mov_b32 exec_hi, 0xfff\n\t"
        "buffer_load_dwordx4 %[xo1], %[xr], s60 offen lds\n\t"
        "s_mov_b32 exec_hi, -1\n\t"
        "ds_read_b64_tr_b16 a[216:217], %[wa00] offset:18432\n\t"
        "v_mfma_f32_16x16x32_bf16 v[56:59], a[196:199], v[140:143], v[56:59]\n\t"
        "ds_read_b64_tr_b16 a[218:219], %[wa01] offset:18432\n\t"
        "ds_read_b64_tr_b16 a[220:221], %[wa10] offset:18432\n\t"
        "v_mfma_f32_16x16x32_bf16 v[60:63], a[200:203], v[140:143], v[60:63]\n\t"
        "ds_read_b64_tr_b16 a[222:223], %[wa11] offset:18432\n\t"
        "ds_read_b64_tr_b16 a[224:225], %[wa20] offset:18432\n\t"
        "v_mfma_f32_16x16x32_bf16 v[40:43], a[204:207], v[140:143], v[40:43]\n\t"
        "ds_read_b64_tr_b16 a[226:227], %[wa21] offset:18432\n\t"
        "ds_read_b64_tr_b16 a[228:229], %[wa00] offset:24576\n\t"
        "v_mfma_f32_16x16x32_bf16 v[44:47], a[208:211], v[140:143], v[44:47]\n\t"
        "ds_read_b64_tr_b16 a[230:231], %[wa01] offset:24576\n\t"
        "ds_read_b64_tr_b16 a[232:233], %[wa10] offset:24576\n\t"
        "v_mfma_f32_16x16x32_bf16 v[48:51], a[212:215], v[140:143], v[48:51]\n\t"
        "ds_read_b64_tr_b16 a[234:235], %[wa11] offset:24576\n\t"
        "ds_read_b64_tr_b16 a[236:237], %[wa20] offset:24576\n\t"
        "v_mfma_f32_16x16x32_bf16 v[52:55], a[204:207], v[144:147], v[52:55]\n\t"
        "ds_read_b64 v[132:133], %[xa2] offset:13856\n\t"
        "ds_read_b64 v[134:135], v242 offset:13856\n\t"
        "ds_read_b64_tr_b16 a[238:239], %[wa21] offset:24576\n\t"
        "v_mfma_f32_16x16x32_bf16 v[56:59], a[208:211], v[144:147], v[56:59]\n\t"
        "ds_read_b64_tr_b16 a[240:241], %[wa00] offset:30720\n\t"
        "ds_read_b64_tr_b16 a[242:243], %[wa01] offset:30720\n\t"
        "v_mfma_f32_16x16x32_bf16 v[60:63], a[212:215], v[144:147], v[60:63]\n\t"
        "ds_read_b64_tr_b16 a[244:245], %[wa10] offset:30720\n\t"
        "ds_read_b64_tr_b16 a[246:247], %[wa11] offset:30720\n\t"
        "s_waitcnt lgkmcnt(15)\n\t"
        "v_mfma_f32_16x16x32_bf16 v[40:43], a[216:219], v[124:127], v[40:43]\n\t"
        "ds_read_b64 v[136:137], %[xa2] offset:20784\n\t"
        "ds_read_b64 v[138:139], v242 offset:20784\n\t"
        "ds_read_b64_tr_b16 a[248:249], %[wa20] offset:30720\n\t"
        "s_waitcnt lgkmcnt(15)\n\t"
        "v_mfma_f32_16x16x32_bf16 v[44:47], a[220:223], v[124:127], v[44:47]\n\t"
        "ds_read_b64_tr_b16 a[250:251], %[wa21] offset:30720\n\t"
        "s_waitcnt lgkmcnt(15)\n\t"
        "v_mfma_f32_16x16x32_bf16 v[48:51], a[224:227], v[124:127], v[48:51]\n\t"
        "v_mfma_f32_16x16x32_bf16 v[52:55], a[216:219], v[128:131], v[52:55]\n\t"
        "ds_read_b64 v[140:141], %[xa2] offset:64\n\t"
        "ds_read_b64 v[142:143], v242 offset:64\n\t"
        "v_mfma_f32_16x16x32_bf16 v[56:59], a[220:223], v[128:131], v[56:59]\n\t"
        "v_mfma_f32_16x16x32_bf16 v[60:63], a[224:227], v[128:131], v[60:63]\n\t"
        "s_waitcnt lgkmcnt(15)\n\t"
        "v_mfma_f32_16x16x32_bf16 v[40:43], a[228:231], v[128:131], v[40:43]\n\t"
        "s_waitcnt lgkmcnt(14)\n\t"
        "v_mfma_f32_16x16x32_bf16 v[44:47], a[232:235], v[128:131], v[44:47]\n\t"
        "s_waitcnt lgkmcnt(10)\n\t"
        "v_mfma_f32_16x16x32_bf16 v[48:51], a[236:239], v[128:131], v[48:51]\n\t"
        "ds_read_b64 v[144:145], %[xa2] offset:6992\n\t"
        "ds_read_b64 v[146:147], v242 offset:6992\n\t"
        "s_waitcnt vmcnt(15)\n\t"
        "s_waitcnt lgkmcnt(4)\n\t"
        "s_barrier\n\t"
        "v_mfma_f32_16x16x32_bf16 v[52:55], a[228:231], v[132:135], v[52:55]\n\t"
        "ds_read_b64_tr_b16 a[252:253], %[wa00] offset:36864\n\t"
        "ds_read_b64_tr_b16 a[254:255], %[wa01] offset:36864\n\t"
        "v_mfma_f32_16x16x32_bf16 v[56:59], a[232:235], v[132:135], v[56:59]\n\t"
        "ds_read_b64_tr_b16 v[148:149], %[wa10] offset:36864\n\t"
        "ds_read_b64_tr_b16 v[150:151], %[wa11] offset:36864\n\t"
        "v_mfma_f32_16x16x32_bf16 v[60:63], a[236:239], v[132:135], v[60:63]\n\t"
        "ds_read_b64_tr_b16 v[152:153], %[wa20] offset:36864\n\t"
        "ds_read_b64_tr_b16 v[154:155], %[wa21] offset:36864\n\t"
        "v_mfma_f32_16x16x32_bf16 v[40:43], a[240:243], v[132:135], v[40:43]\n\t"
        "ds_read_b64_tr_b16 v[156:157], %[wa00] offset:43008\n\t"
        "ds_read_b64_tr_b16 v[158:159], %[wa01] offset:43008\n\t"
        "v_mfma_f32_16x16x32_bf16 v[44:47], a[244:247], v[132:135], v[44:47]\n\t"
        "ds_read_b64_tr_b16 v[160:161], %[wa10] offset:43008\n\t"
        "ds_read_b64_tr_b16 v[162:163], %[wa11] offset:43008\n\t"
        "v_mfma_f32_16x16x32_bf16 v[48:51], a[248:251], v[132:135], v[48:51]\n\t"
        "ds_read_b64_tr_b16 v[164:165], %[wa20] offset:43008\n\t"
        "ds_read_b64_tr_b16 v[166:167], %[wa21] offset:43008\n\t"
        "v_mfma_f32_16x16x32_bf16 v[52:55], a[240:243], v[136:139], v[52:55]\n\t"
        "ds_read_b64 v[124:125], %[xa2] offset:13920\n\t"
        "ds_read_b64 v[126:127], v242 offset:13920\n\t"
        "ds_read_b64_tr_b16 v[168:169], %[wa00] offset:49152\n\t"
        "v_mfma_f32_16x16x32_bf16 v[56:59], a[244:247], v[136:139], v[56:59]\n\t"
        "ds_read_b64_tr_b16 v[170:171], %[wa01] offset:49152\n\t"
        "ds_read_b64_tr_b16 v[172:173], %[wa10] offset:49152\n\t"
        "v_mfma_f32_16x16x32_bf16 v[60:63], a[248:251], v[136:139], v[60:63]\n\t"
        "ds_read_b64_tr_b16 v[174:175], %[wa11] offset:49152\n\t"
        "ds_read_b64_tr_b16 v[176:177], %[wa20] offset:49152\n\t"
        "s_waitcnt lgkmcnt(15)\n\t"
        "v_mfma_f32_16x16x32_bf16 v[40:43], a[252:255], v[140:143], v[40:43]\n\t"
        "ds_read_b64 v[128:129], %[xa2] offset:20848\n\t"
        "ds_read_b64 v[130:131], v242 offset:20848\n\t"
        "ds_read_b64_tr_b16 v[178:179], %[wa21] offset:49152\n\t"
        "s_waitcnt lgkmcnt(15)\n\t"
        "v_mfma_f32_16x16x32_bf16 v[44:47], v[148:151], v[140:143], v[44:47]\n\t"
        "s_waitcnt lgkmcnt(15)\n\t"
        "v_mfma_f32_16x16x32_bf16 v[48:51], v[152:155], v[140:143], v[48:51]\n\t"
        "v_mfma_f32_16x16x32_bf16 v[52:55], a[252:255], v[144:147], v[52:55]\n\t"
        "ds_read_b64 v[132:133], %[xa2] offset:128\n\t"
        "ds_read_b64 v[134:135], v242 offset:128\n\t"
        "v_mfma_f32_16x16x32_bf16 v[56:59], v[148:151], v[144:147], v[56:59]\n\t"
        "v_mfma_f32_16x16x32_bf16 v[60:63], v[152:155], v[144:147], v[60:63]\n\t"
        "s_waitcnt lgkmcnt(15)\n\t"
        "v_mfma_f32_16x16x32_bf16 v[40:43], v[156:159], v[144:147], v[40:43]\n\t"
        "s_waitcnt lgkmcnt(14)\n\t"
        "v_mfma_f32_16x16x32_bf16 v[44:47], v[160:163], v[144:147], v[44:47]\n\t"
        "s_waitcnt lgkmcnt(12)\n\t"
        "v_mfma_f32_16x16x32_bf16 v[48:51], v[164:167], v[144:147], v[48:51]\n\t"
        "ds_read_b64 v[136:137], %[xa2] offset:7056\n\t"
        "ds_read_b64 v[138:139], v242 offset:7056\n\t"
        "s_waitcnt vmcnt(10)\n\t"
        "s_waitcnt lgkmcnt(4)\n\t"
        "s_barrier\n\t"
        "v_mfma_f32_16x16x32_bf16 v[52:55], v[156:159], v[124:127], v[52:55]\n\t"
        "ds_read_b64_tr_b16 v[180:181], v243 offset:0\n\t"
        "ds_read_b64_tr_b16 v[182:183], v244 offset:0\n\t"
        "v_mfma_f32_16x16x32_bf16 v[56:59], v[160:163], v[124:127], v[56:59]\n\t"
        "ds_read_b64_tr_b16 v[184:185], v245 offset:0\n\t"
        "ds_read_b64_tr_b16 v[186:187], v246 offset:0\n\t"
        "v_mfma_f32_16x16x32_bf16 v[60:63], v[164:167], v[124:127], v[60:63]\n\t"
        "ds_read_b64_tr_b16 v[188:189], v247 offset:0\n\t"
        "ds_read_b64_tr_b16 v[190:191], v248 offset:0\n\t"
        "v_mfma_f32_16x16x32_bf16 v[40:43], v[168:171], v[124:127], v[40:43]\n\t"
        "ds_read_b64_tr_b16 v[192:193], v243 offset:6144\n\t"
        "ds_read_b64_tr_b16 v[194:195], v244 offset:6144\n\t"
        "v_mfma_f32_16x16x32_bf16 v[44:47], v[172:175], v[124:127], v[44:47]\n\t"
        "ds_read_b64_tr_b16 v[196:197], v245 offset:6144\n\t"
        "ds_read_b64_tr_b16 v[198:199], v246 offset:6144\n\t"
        "v_mfma_f32_16x16x32_bf16 v[48:51], v[176:179], v[124:127], v[48:51]\n\t"
        "ds_read_b64_tr_b16 v[200:201], v247 offset:6144\n\t"
        "ds_read_b64_tr_b16 v[202:203], v248 offset:6144\n\t"
        "v_mfma_f32_16x16x32_bf16 v[52:55], v[168:171], v[128:131], v[52:55]\n\t"
        "ds_read_b64 v[140:141], %[xa2] offset:13984\n\t"
        "ds_read_b64 v[142:143], v242 offset:13984\n\t"
        "ds_read_b64_tr_b16 v[204:205], v243 offset:12288\n\t"
        "v_mfma_f32_16x16x32_bf16 v[56:59], v[172:175], v[128:131], v[56:59]\n\t"
        "ds_read_b64_tr_b16 v[206:207], v244 offset:12288\n\t"
        "ds_read_b64_tr_b16 v[208:209], v245 offset:12288\n\t"
        "v_mfma_f32_16x16x32_bf16 v[60:63], v[176:179], v[128:131], v[60:63]\n\t"
        "ds_read_b64_tr_b16 v[210:211], v246 offset:12288\n\t"
        "ds_read_b64_tr_b16 v[212:213], v247 offset:12288\n\t"
        "s_waitcnt lgkmcnt(15)\n\t"
        "v_mfma_f32_16x16x32_bf16 v[40:43], v[180:183], v[132:135], v[40:43]\n\t"
        "ds_read_b64 v[144:145], %[xa2] offset:20912\n\t"
        "ds_read_b64 v[146:147], v242 offset:20912\n\t"
        "ds_read_b64_tr_b16 v[214:215], v248 offset:12288\n\t"
        "s_waitcnt lgkmcnt(15)\n\t"
        "v_mfma_f32_16x16x32_bf16 v[44:47], v[184:187], v[132:135], v[44:47]\n\t"
        "s_waitcnt lgkmcnt(15)\n\t"
        "v_mfma_f32_16x16x32_bf16 v[48:51], v[188:191], v[132:135], v[48:51]\n\t"
        "v_mfma_f32_16x16x32_bf16 v[52:55], v[180:183], v[136:139], v[52:55]\n\t"
        "ds_read_b64 v[124:125], %[xa0] offset:13856\n\t"
        "ds_read_b64 v[126:127], v240 offset:13856\n\t"
        "v_mfma_f32_16x16x32_bf16 v[56:59], v[184:187], v[136:139], v[56:59]\n\t"
        "v_mfma_f32_16x16x32_bf16 v[60:63], v[188:191], v[136:139], v[60:63]\n\t"
        "s_waitcnt lgkmcnt(15)\n\t"
        "v_mfma_f32_16x16x32_bf16 v[40:43], v[192:195], v[136:139], v[40:43]\n\t"
        "s_waitcnt lgkmcnt(14)\n\t"
        "v_mfma_f32_16x16x32_bf16 v[44:47], v[196:199], v[136:139], v[44:47]\n\t"
        "s_waitcnt lgkmcnt(12)\n\t"
        "v_mfma_f32_16x16x32_bf16 v[48:51], v[200:203], v[136:139], v[48:51]\n\t"
        "ds_read_b64 v[128:129], %[xa0] offset:20784\n\t"
        "ds_read_b64 v[130:131], v240 offset:20784\n\t"
        "s_waitcnt vmcnt(6)\n\t"
        "s_waitcnt lgkmcnt(4)\n\t"
        "s_barrier\n\t"
        "v_mfma_f32_16x16x32_bf16 v[52:55], v[192:195], v[140:143], v[52:55]\n\t"
        "v_mfma_f32_16x16x32_bf16 v[56:59], v[196:199], v[140:143], v[56:59]\n\t"
        "v_mfma_f32_16x16x32_bf16 v[60:63], v[200:203], v[140:143], v[60:63]\n\t"
        "v_mfma_f32_16x16x32_bf16 v[40:43], v[204:207], v[140:143], v[40:43]\n\t"
        "v_mfma_f32_16x16x32_bf16 v[44:47], v[208:211], v[140:143], v[44:47]\n\t"
        "v_mfma_f32_16x16x32_bf16 v[48:51], v[212:215], v[140:143], v[48:51]\n\t"
        "v_mfma_f32_16x16x32_bf16 v[52:55], v[204:207], v[144:147], v[52:55]\n\t"
        "ds_read_b64 v[132:133], %[xa0] offset:27712\n\t"
        "ds_read_b64 v[134:135], v240 offset:27712\n\t"
        "v_mfma_f32_16x16x32_bf16 v[56:59], v[208:211], v[144:147], v[56:59]\n\t"
        "v_mfma_f32_16x16x32_bf16 v[60:63], v[212:215], v[144:147], v[60:63]\n\t"
        "s_waitcnt lgkmcnt(4)\n\t"
        "v_mfma_f32_16x16x32_bf16 v[64:67], a[0:3], v[124:127], 0\n\t"
        "ds_read_b64 v[136:137], %[xa0] offset:34640\n\t"
        "ds_read_b64 v[138:139], v240 offset:34640\n\t"
        "v_mfma_f32_16x16x32_bf16 v[68:71], a[4:7], v[124:127], 0\n\t"
        "v_mfma_f32_16x16x32_bf16 v[72:75], a[8:11], v[124:127], 0\n\t"
        "s_waitcnt lgkmcnt(4)\n\t"
        "v_mfma_f32_16x16x32_bf16 v[76:79], a[0:3], v[128:131], 0\n\t"
        "ds_read_b64 v[140:141], %[xa0] offset:13920\n\t"
        "ds_read_b64 v[142:143], v240 offset:13920\n\t"
        "v_mfma_f32_16x16x32_bf16 v[80:83], a[4:7], v[128:131], 0\n\t"
        "v_cvt_pk_bf16_f32 v216, v40, v41\n\t"
        "v_cvt_pk_bf16_f32 v217, v42, v43\n\t"
        "v_mfma_f32_16x16x32_bf16 v[84:87], a[8:11], v[128:131], 0\n\t"
        "v_cvt_pk_bf16_f32 v218, v44, v45\n\t"
        "v_cvt_pk_bf16_f32 v219, v46, v47\n\t"
        "v_mfma_f32_16x16x32_bf16 v[64:67], a[12:15], v[128:131], v[64:67]\n\t"
        "s_nop 0\n\t"
        "v_mfma_f32_16x16x32_bf16 v[68:71], a[16:19], v[128:131], v[68:71]\n\t"
        "v_permlane16_swap_b32 v216, v218\n\t"
        "v_permlane16_swap_b32 v217, v219\n\t"
        "v_mfma_f32_16x16x32_bf16 v[72:75], a[20:23], v[128:131], v[72:75]\n\t"
        "ds_write_b128 %[sw1], v[216:219] offset:0\n\t"
        "s_waitcnt lgkmcnt(5)\n\t"
        "v_mfma_f32_16x16x32_bf16 v[76:79], a[12:15], v[132:135], v[76:79]\n\t"
        "ds_read_b64 v[144:145], %[xa0] offset:20848\n\t"
        "ds_read_b64 v[146:147], v240 offset:20848\n\t"
        "v_mfma_f32_16x16x32_bf16 v[80:83], a[16:19], v[132:135], v[80:83]\n\t"
        "v_cvt_pk_bf16_f32 v220, v52, v53\n\t"
        "v_cvt_pk_bf16_f32 v221, v54, v55\n\t"
        "v_mfma_f32_16x16x32_bf16 v[84:87], a[20:23], v[132:135], v[84:87]\n\t"
        "v_cvt_pk_bf16_f32 v222, v56, v57\n\t"
        "v_cvt_pk_bf16_f32 v223, v58, v59\n\t"
        "v_mfma_f32_16x16x32_bf16 v[64:67], a[24:27], v[132:135], v[64:67]\n\t"
        "s_nop 0\n\t"
        "v_mfma_f32_16x16x32_bf16 v[68:71], a[28:31], v[132:135], v[68:71]\n\t"
        "v_permlane16_swap_b32 v220, v222\n\t"
        "v_permlane16_swap_b32 v221, v223\n\t"
        "v_mfma_f32_16x16x32_bf16 v[72:75], a[32:35], v[132:135], v[72:75]\n\t"
        "ds_write_b128 %[sw1], v[220:223] offset:6656\n\t"
        "s_waitcnt lgkmcnt(6)\n\t"
        "v_mfma_f32_16x16x32_bf16 v[76:79], a[24:27], v[136:139], v[76:79]\n\t"
        "ds_read_b64 v[124:125], %[xa0] offset:27776\n\t"
        "ds_read_b64 v[126:127], v240 offset:27776\n\t"
        "v_mfma_f32_16x16x32_bf16 v[80:83], a[28:31], v[136:139], v[80:83]\n\t"
        "v_cvt_pk_bf16_f32 v224, v48, v49\n\t"
        "v_cvt_pk_bf16_f32 v225, v50, v51\n\t"
        "v_mfma_f32_16x16x32_bf16 v[84:87], a[32:35], v[136:139], v[84:87]\n\t"
        "v_cvt_pk_bf16_f32 v226, v60, v61\n\t"
        "v_cvt_pk_bf16_f32 v227, v62, v63\n\t"
        "s_waitcnt lgkmcnt(6)\n\t"
        "v_mfma_f32_16x16x32_bf16 v[64:67], a[36:39], v[140:143], v[64:67]\n\t"
        "ds_read_b64 v[128:129], %[xa0] offset:34704\n\t"
        "ds_read_b64 v[130:131], v240 offset:34704\n\t"
        "v_mfma_f32_16x16x32_bf16 v[68:71], a[40:43], v[140:143], v[68:71]\n\t"
        "s_nop 0\n\t"
        "v_mfma_f32_16x16x32_bf16 v[72:75], a[44:47], v[140:143], v[72:75]\n\t"
        "v_permlane16_swap_b32 v224, v226\n\t"
        "v_permlane16_swap_b32 v225, v227\n\t"
        "s_waitcnt lgkmcnt(5)\n\t"
        "v_mfma_f32_16x16x32_bf16 v[76:79], a[36:39], v[144:147], v[76:79]\n\t"
        "ds_read_b64 v[132:133], %[xa0] offset:13984\n\t"
        "ds_read_b64 v[134:135], v240 offset:13984\n\t"
        "v_mfma_f32_16x16x32_bf16 v[80:83], a[40:43], v[144:147], v[80:83]\n\t"
        "ds_write_b128 %[sw2], v[224:227] offset:0\n\t"
        "v_mfma_f32_16x16x32_bf16 v[84:87], a[44:47], v[144:147], v[84:87]\n\t"
        "v_mfma_f32_16x16x32_bf16 v[64:67], a[48:51], v[144:147], v[64:67]\n\t"
        "s_waitcnt lgkmcnt(0)\n\t"
        "s_barrier\n\t"
        "v_mfma_f32_16x16x32_bf16 v[68:71], a[52:55], v[144:147], v[68:71]\n\t"
        "ds_read_b128 v[228:231], %[rl0] offset:0\n\t"
        "v_mfma_f32_16x16x32_bf16 v[72:75], a[56:59], v[144:147], v[72:75]\n\t"
        "ds_read_b128 v[232:235], %[rl1] offset:0\n\t"
        "v_mfma_f32_16x16x32_bf16 v[76:79], a[48:51], v[124:127], v[76:79]\n\t"
        "ds_read_b64 v[136:137], %[xa0] offset:20912\n\t"
        "ds_read_b64 v[138:139], v240 offset:20912\n\t"
        "v_mfma_f32_16x16x32_bf16 v[80:83], a[52:55], v[124:127], v[80:83]\n\t"
        "ds_read_b128 v[236:239], %[rl2] offset:0\n\t"
        "v_mfma_f32_16x16x32_bf16 v[84:87], a[56:59], v[124:127], v[84:87]\n\t"
        "s_add_u32 s62, %[ob], 0\n\t"
        "v_mfma_f32_16x16x32_bf16 v[64:67], a[60:63], v[124:127], v[64:67]\n\t"
        "v_mfma_f32_16x16x32_bf16 v[68:71], a[64:67], v[124:127], v[68:71]\n\t"
        "v_mfma_f32_16x16x32_bf16 v[72:75], a[68:71], v[124:127], v[72:75]\n\t"
        "s_waitcnt lgkmcnt(4)\n\t"
        "buffer_store_dwordx4 v[228:231], %[go0], %[orr], s62 offen sc1\n\t"
        "v_mfma_f32_16x16x32_bf16 v[76:79], a[60:63], v[128:131], v[76:79]\n\t"
        "ds_read_b64 v[140:141], %[xa0] offset:27840\n\t"
        "ds_read_b64 v[142:143], v240 offset:27840\n\t"
        "v_mfma_f32_16x16x32_bf16 v[80:83], a[64:67], v[128:131], v[80:83]\n\t"
        "s_waitcnt lgkmcnt(5)\n\t"
        "buffer_store_dwordx4 v[232:235], %[go1], %[orr], s62 offen sc1\n\t"
        "v_mfma_f32_16x16x32_bf16 v[84:87], a[68:71], v[128:131], v[84:87]\n\t"
        "s_waitcnt lgkmcnt(2)\n\t"
        "buffer_store_dwordx4 v[236:239], %[go2], %[orr], s62 offen sc1\n\t"
        "v_mfma_f32_16x16x32_bf16 v[64:67], a[72:75], v[132:135], v[64:67]\n\t"
        "ds_read_b64 v[144:145], %[xa0] offset:34768\n\t"
        "ds_read_b64 v[146:147], v240 offset:34768\n\t"
        "v_mfma_f32_16x16x32_bf16 v[68:71], a[76:79], v[132:135], v[68:71]\n\t"
        "v_mfma_f32_16x16x32_bf16 v[72:75], a[80:83], v[132:135], v[72:75]\n\t"
        "v_mfma_f32_16x16x32_bf16 v[76:79], a[72:75], v[136:139], v[76:79]\n\t"
        "ds_read_b64 v[124:125], %[xa1] offset:13856\n\t"
        "ds_read_b64 v[126:127], v241 offset:13856\n\t"
        "v_mfma_f32_16x16x32_bf16 v[80:83], a[76:79], v[136:139], v[80:83]\n\t"
        "v_mfma_f32_16x16x32_bf16 v[84:87], a[80:83], v[136:139], v[84:87]\n\t"
        "v_mfma_f32_16x16x32_bf16 v[64:67], a[84:87], v[136:139], v[64:67]\n\t"
        "v_mfma_f32_16x16x32_bf16 v[68:71], a[88:91], v[136:139], v[68:71]\n\t"
        "v_mfma_f32_16x16x32_bf16 v[72:75], a[92:95], v[136:139], v[72:75]\n\t"
        "s_waitcnt lgkmcnt(4)\n\t"
        "v_mfma_f32_16x16x32_bf16 v[76:79], a[84:87], v[140:143], v[76:79]\n\t"
        "ds_read_b64 v[128:129], %[xa1] offset:20784\n\t"
        "ds_read_b64 v[130:131], v241 offset:20784\n\t"
        "v_mfma_f32_16x16x32_bf16 v[80:83], a[88:91], v[140:143], v[80:83]\n\t"
        "v_mfma_f32_16x16x32_bf16 v[84:87], a[92:95], v[140:143], v[84:87]\n\t"
        "v_mfma_f32_16x16x32_bf16 v[64:67], a[96:99], v[140:143], v[64:67]\n\t"
        "v_mfma_f32_16x16x32_bf16 v[68:71], a[100:103], v[140:143], v[68:71]\n\t"
        "v_mfma_f32_16x16x32_bf16 v[72:75], a[104:107], v[140:143], v[72:75]\n\t"
        "s_waitcnt lgkmcnt(4)\n\t"
        "v_mfma_f32_16x16x32_bf16 v[76:79], a[96:99], v[144:147], v[76:79]\n\t"
        "ds_read_b64 v[132:133], %[xa1] offset:27712\n\t"
        "ds_read_b64 v[134:135], v241 offset:27712\n\t"
        "v_mfma_f32_16x16x32_bf16 v[80:83], a[100:103], v[144:147], v[80:83]\n\t"
        "v_mfma_f32_16x16x32_bf16 v[84:87], a[104:107], v[144:147], v[84:87]\n\t"
        "s_waitcnt lgkmcnt(4)\n\t"
        "v_mfma_f32_16x16x32_bf16 v[64:67], a[108:111], v[124:127], v[64:67]\n\t"
        "ds_read_b64 v[136:137], %[xa1] offset:34640\n\t"
        "ds_read_b64 v[138:139], v241 offset:34640\n\t"
        "v_mfma_f32_16x16x32_bf16 v[68:71], a[112:115], v[124:127], v[68:71]\n\t"
        "v_mfma_f32_16x16x32_bf16 v[72:75], a[116:119], v[124:127], v[72:75]\n\t"
        "s_waitcnt lgkmcnt(4)\n\t"
        "v_mfma_f32_16x16x32_bf16 v[76:79], a[108:111], v[128:131], v[76:79]\n\t"
        "ds_read_b64 v[140:141], %[xa1] offset:13920\n\t"
        "ds_read_b64 v[142:143], v241 offset:13920\n\t"
        "v_mfma_f32_16x16x32_bf16 v[80:83], a[112:115], v[128:131], v[80:83]\n\t"
        "v_mfma_f32_16x16x32_bf16 v[84:87], a[116:119], v[128:131], v[84:87]\n\t"
        "v_mfma_f32_16x16x32_bf16 v[64:67], a[120:123], v[128:131], v[64:67]\n\t"
        "v_mfma_f32_16x16x32_bf16 v[68:71], a[124:127], v[128:131], v[68:71]\n\t"
        "v_mfma_f32_16x16x32_bf16 v[72:75], a[128:131], v[128:131], v[72:75]\n\t"
        "s_waitcnt lgkmcnt(4)\n\t"
        "v_mfma_f32_16x16x32_bf16 v[76:79], a[120:123], v[132:135], v[76:79]\n\t"
        "ds_read_b64 v[144:145], %[xa1] offset:20848\n\t"
        "ds_read_b64 v[146:147], v241 offset:20848\n\t"
        "v_mfma_f32_16x16x32_bf16 v[80:83], a[124:127], v[132:135], v[80:83]\n\t"
        "v_mfma_f32_16x16x32_bf16 v[84:87], a[128:131], v[132:135], v[84:87]\n\t"
        "v_mfma_f32_16x16x32_bf16 v[64:67], a[132:135], v[132:135], v[64:67]\n\t"
        "v_mfma_f32_16x16x32_bf16 v[68:71], a[136:139], v[132:135], v[68:71]\n\t"
        "v_mfma_f32_16x16x32_bf16 v[72:75], a[140:143], v[132:135], v[72:75]\n\t"
        "s_waitcnt lgkmcnt(4)\n\t"
        "v_mfma_f32_16x16x32_bf16 v[76:79], a[132:135], v[136:139], v[76:79]\n\t"
        "ds_read_b64 v[124:125], %[xa1] offset:27776\n\t"
        "ds_read_b64 v[126:127], v241 offset:27776\n\t"
        "v_mfma_f32_16x16x32_bf16 v[80:83], a[136:139], v[136:139], v[80:83]\n\t"
        "v_mfma_f32_16x16x32_bf16 v[84:87], a[140:143], v[136:139], v[84:87]\n\t"
        "s_waitcnt lgkmcnt(4)\n\t"
        "v_mfma_f32_16x16x32_bf16 v[64:67], a[144:147], v[140:143], v[64:67]\n\t"
        "ds_read_b64 v[128:129], %[xa1] offset:34704\n\t"
        "ds_read_b64 v[130:131], v241 offset:34704\n\t"
        "v_mfma_f32_16x16x32_bf16 v[68:71], a[148:151], v[140:143], v[68:71]\n\t"
        "v_mfma_f32_16x16x32_bf16 v[72:75], a[152:155], v[140:143], v[72:75]\n\t"
        "s_waitcnt lgkmcnt(4)\n\t"
        "v_mfma_f32_16x16x32_bf16 v[76:79], a[144:147], v[144:147], v[76:79]\n\t"
        "ds_read_b64 v[132:133], %[xa1] offset:13984\n\t"
        "ds_read_b64 v[134:135], v241 offset:13984\n\t"
        "v_mfma_f32_16x16x32_bf16 v[80:83], a[148:151], v[144:147], v[80:83]\n\t"
        "v_mfma_f32_16x16x32_bf16 v[84:87], a[152:155], v[144:147], v[84:87]\n\t"
        "v_mfma_f32_16x16x32_bf16 v[64:67], a[156:159], v[144:147], v[64:67]\n\t"
        "v_mfma_f32_16x16x32_bf16 v[68:71], a[160:163], v[144:147], v[68:71]\n\t"
        "v_mfma_f32_16x16x32_bf16 v[72:75], a[164:167], v[144:147], v[72:75]\n\t"
        "s_waitcnt lgkmcnt(4)\n\t"
        "v_mfma_f32_16x16x32_bf16 v[76:79], a[156:159], v[124:127], v[76:79]\n\t"
        "ds_read_b64 v[136:137], %[xa1] offset:20912\n\t"
        "ds_read_b64 v[138:139], v241 offset:20912\n\t"
        "v_mfma_f32_16x16x32_bf16 v[80:83], a[160:163], v[124:127], v[80:83]\n\t"
        "v_mfma_f32_16x16x32_bf16 v[84:87], a[164:167], v[124:127], v[84:87]\n\t"
        "v_mfma_f32_16x16x32_bf16 v[64:67], a[168:171], v[124:127], v[64:67]\n\t"
        "v_mfma_f32_16x16x32_bf16 v[68:71], a[172:175], v[124:127], v[68:71]\n\t"
        "v_mfma_f32_16x16x32_bf16 v[72:75], a[176:179], v[124:127], v[72:75]\n\t"
        "s_waitcnt lgkmcnt(4)\n\t"
        "v_mfma_f32_16x16x32_bf16 v[76:79], a[168:171], v[128:131], v[76:79]\n\t"
        "ds_read_b64 v[140:141], %[xa1] offset:27840\n\t"
        "ds_read_b64 v[142:143], v241 offset:27840\n\t"
        "v_mfma_f32_16x16x32_bf16 v[80:83], a[172:175], v[128:131], v[80:83]\n\t"
        "v_mfma_f32_16x16x32_bf16 v[84:87], a[176:179], v[128:131], v[84:87]\n\t"
        "s_waitcnt lgkmcnt(4)\n\t"
        "v_mfma_f32_16x16x32_bf16 v[64:67], a[180:183], v[132:135], v[64:67]\n\t"
        "ds_read_b64 v[144:145], %[xa1] offset:34768\n\t"
        "ds_read_b64 v[146:147], v241 offset:34768\n\t"
        "v_mfma_f32_16x16x32_bf16 v[68:71], a[184:187], v[132:135], v[68:71]\n\t"
        "v_mfma_f32_16x16x32_bf16 v[72:75], a[188:191], v[132:135], v[72:75]\n\t"
        "s_waitcnt lgkmcnt(4)\n\t"
        "v_mfma_f32_16x16x32_bf16 v[76:79], a[180:183], v[136:139], v[76:79]\n\t"
        "ds_read_b64 v[124:125], %[xa2] offset:13856\n\t"
        "ds_read_b64 v[126:127], v242 offset:13856\n\t"
        "v_mfma_f32_16x16x32_bf16 v[80:83], a[184:187], v[136:139], v[80:83]\n\t"
        "v_mfma_f32_16x16x32_bf16 v[84:87], a[188:191], v[136:139], v[84:87]\n\t"
        "v_mfma_f32_16x16x32_bf16 v[64:67], a[192:195], v[136:139], v[64:67]\n\t"
        "v_mfma_f32_16x16x32_bf16 v[68:71], a[196:199], v[136:139], v[68:71]\n\t"
        "v_mfma_f32_16x16x32_bf16 v[72:75], a[200:203], v[136:139], v[72:75]\n\t"
        "s_waitcnt lgkmcnt(4)\n\t"
        "v_mfma_f32_16x16x32_bf16 v[76:79], a[192:195], v[140:143], v[76:79]\n\t"
        "ds_read_b64 v[128:129], %[xa2] offset:20784\n\t"
        "ds_read_b64 v[130:131], v242 offset:20784\n\t"
        "v_mfma_f32_16x16x32_bf16 v[80:83], a[196:199], v[140:143], v[80:83]\n\t"
        "v_mfma_f32_16x16x32_bf16 v[84:87], a[200:203], v[140:143], v[84:87]\n\t"
        "v_mfma_f32_16x16x32_bf16 v[64:67], a[204:207], v[140:143], v[64:67]\n\t"
        "v_mfma_f32_16x16x32_bf16 v[68:71], a[208:211], v[140:143], v[68:71]\n\t"
        "v_mfma_f32_16x16x32_bf16 v[72:75], a[212:215], v[140:143], v[72:75]\n\t"
        "s_waitcnt lgkmcnt(4)\n\t"
        "v_mfma_f32_16x16x32_bf16 v[76:79], a[204:207], v[144:147], v[76:79]\n\t"
        "ds_read_b64 v[132:133], %[xa2] offset:27712\n\t"
        "ds_read_b64 v[134:135], v242 offset:27712\n\t"
        "v_mfma_f32_16x16x32_bf16 v[80:83], a[208:211], v[144:147], v[80:83]\n\t"
        "v_mfma_f32_16x16x32_bf16 v[84:87], a[212:215], v[144:147], v[84:87]\n\t"
        "s_waitcnt lgkmcnt(4)\n\t"
        "v_mfma_f32_16x16x32_bf16 v[64:67], a[216:219], v[124:127], v[64:67]\n\t"
        "ds_read_b64 v[136:137], %[xa2] offset:34640\n\t"
        "ds_read_b64 v[138:139], v242 offset:34640\n\t"
        "v_mfma_f32_16x16x32_bf16 v[68:71], a[220:223], v[124:127], v[68:71]\n\t"
        "v_mfma_f32_16x16x32_bf16 v[72:75], a[224:227], v[124:127], v[72:75]\n\t"
        "s_waitcnt lgkmcnt(4)\n\t"
        "v_mfma_f32_16x16x32_bf16 v[76:79], a[216:219], v[128:131], v[76:79]\n\t"
        "ds_read_b64 v[140:141], %[xa2] offset:13920\n\t"
        "ds_read_b64 v[142:143], v242 offset:13920\n\t"
        "v_mfma_f32_16x16x32_bf16 v[80:83], a[220:223], v[128:131], v[80:83]\n\t"
        "v_mfma_f32_16x16x32_bf16 v[84:87], a[224:227], v[128:131], v[84:87]\n\t"
        "v_mfma_f32_16x16x32_bf16 v[64:67], a[228:231], v[128:131], v[64:67]\n\t"
        "v_mfma_f32_16x16x32_bf16 v[68:71], a[232:235], v[128:131], v[68:71]\n\t"
        "v_mfma_f32_16x16x32_bf16 v[72:75], a[236:239], v[128:131], v[72:75]\n\t"
        "s_waitcnt lgkmcnt(4)\n\t"
        "v_mfma_f32_16x16x32_bf16 v[76:79], a[228:231], v[132:135], v[76:79]\n\t"
        "ds_read_b64 v[144:145], %[xa2] offset:20848\n\t"
        "ds_read_b64 v[146:147], v242 offset:20848\n\t"
        "v_mfma_f32_16x16x32_bf16 v[80:83], a[232:235], v[132:135], v[80:83]\n\t"
        "v_mfma_f32_16x16x32_bf16 v[84:87], a[236:239], v[132:135], v[84:87]\n\t"
        "v_mfma_f32_16x16x32_bf16 v[64:67], a[240:243], v[132:135], v[64:67]\n\t"
        "v_mfma_f32_16x16x32_bf16 v[68:71], a[244:247], v[132:135], v[68:71]\n\t"
        "v_mfma_f32_16x16x32_bf16 v[72:75], a[248:251], v[132:135], v[72:75]\n\t"
        "s_waitcnt lgkmcnt(4)\n\t"
        "v_mfma_f32_16x16x32_bf16 v[76:79], a[240:243], v[136:139], v[76:79]\n\t"
        "ds_read_b64 v[124:125], %[xa2] offset:27776\n\t"
        "ds_read_b64 v[126:127], v242 offset:27776\n\t"
        "v_mfma_f32_16x16x32_bf16 v[80:83], a[244:247], v[136:139], v[80:83]\n\t"
        "v_mfma_f32_16x16x32_bf16 v[84:87], a[248:251], v[136:139], v[84:87]\n\t"
        "s_waitcnt lgkmcnt(4)\n\t"
        "v_mfma_f32_16x16x32_bf16 v[64:67], a[252:255], v[140:143], v[64:67]\n\t"
        "ds_read_b64 v[128:129], %[xa2] offset:34704\n\t"
        "ds_read_b64 v[130:131], v242 offset:34704\n\t"
        "v_mfma_f32_16x16x32_bf16 v[68:71], v[148:151], v[140:143], v[68:71]\n\t"
        "v_mfma_f32_16x16x32_bf16 v[72:75], v[152:155], v[140:143], v[72:75]\n\t"
        "s_waitcnt lgkmcnt(4)\n\t"
        "v_mfma_f32_16x16x32_bf16 v[76:79], a[252:255], v[144:147], v[76:79]\n\t"
        "ds_read_b64 v[132:133], %[xa2] offset:13984\n\t"
        "ds_read_b64 v[134:135], v242 offset:13984\n\t"
        "v_mfma_f32_16x16x32_bf16 v[80:83], v[148:151], v[144:147], v[80:83]\n\t"
        "v_mfma_f32_16x16x32_bf16 v[84:87], v[152:155], v[144:147], v[84:87]\n\t"
        "v_mfma_f32_16x16x32_bf16 v[64:67], v[156:159], v[144:147], v[64:67]\n\t"
        "v_mfma_f32_16x16x32_bf16 v[68:71], v[160:163], v[144:147], v[68:71]\n\t"
        "v_mfma_f32_16x16x32_bf16 v[72:75], v[164:167], v[144:147], v[72:75]\n\t"
        "s_waitcnt lgkmcnt(4)\n\t"
        "v_mfma_f32_16x16x32_bf16 v[76:79], v[156:159], v[124:127], v[76:79]\n\t"
        "ds_read_b64 v[136:137], %[xa2] offset:20912\n\t"
        "ds_read_b64 v[138:139], v242 offset:20912\n\t"
        "v_mfma_f32_16x16x32_bf16 v[80:83], v[160:163], v[124:127], v[80:83]\n\t"
        "v_mfma_f32_16x16x32_bf16 v[84:87], v[164:167], v[124:127], v[84:87]\n\t"
        "v_mfma_f32_16x16x32_bf16 v[64:67], v[168:171], v[124:127], v[64:67]\n\t"
        "v_mfma_f32_16x16x32_bf16 v[68:71], v[172:175], v[124:127], v[68:71]\n\t"
        "v_mfma_f32_16x16x32_bf16 v[72:75], v[176:179], v[124:127], v[72:75]\n\t"
        "s_waitcnt lgkmcnt(4)\n\t"
        "v_mfma_f32_16x16x32_bf16 v[76:79], v[168:171], v[128:131], v[76:79]\n\t"
        "ds_read_b64 v[140:141], %[xa2] offset:27840\n\t"
        "ds_read_b64 v[142:143], v242 offset:27840\n\t"
        "v_mfma_f32_16x16x32_bf16 v[80:83], v[172:175], v[128:131], v[80:83]\n\t"
        "v_mfma_f32_16x16x32_bf16 v[84:87], v[176:179], v[128:131], v[84:87]\n\t"
        "s_waitcnt lgkmcnt(4)\n\t"
        "v_mfma_f32_16x16x32_bf16 v[64:67], v[180:183], v[132:135], v[64:67]\n\t"
        "ds_read_b64 v[144:145], %[xa2] offset:34768\n\t"
        "ds_read_b64 v[146:147], v242 offset:34768\n\t"
        "v_mfma_f32_16x16x32_bf16 v[68:71], v[184:187], v[132:135], v[68:71]\n\t"
        "v_mfma_f32_16x16x32_bf16 v[72:75], v[188:191], v[132:135], v[72:75]\n\t"
        "s_waitcnt lgkmcnt(4)\n\t"
        "v_mfma_f32_16x16x32_bf16 v[76:79], v[180:183], v[136:139], v[76:79]\n\t"
        "ds_read_b64 v[124:125], %[xa0] offset:27712\n\t"
        "ds_read_b64 v[126:127], v240 offset:27712\n\t"
        "v_mfma_f32_16x16x32_bf16 v[80:83], v[184:187], v[136:139], v[80:83]\n\t"
        "v_mfma_f32_16x16x32_bf16 v[84:87], v[188:191], v[136:139], v[84:87]\n\t"
        "v_mfma_f32_16x16x32_bf16 v[64:67], v[192:195], v[136:139], v[64:67]\n\t"
        "v_mfma_f32_16x16x32_bf16 v[68:71], v[196:199], v[136:139], v[68:71]\n\t"
        "v_mfma_f32_16x16x32_bf16 v[72:75], v[200:203], v[136:139], v[72:75]\n\t"
        "ds_read_b64 v[128:129], %[xa0] offset:34640\n\t"
        "ds_read_b64 v[130:131], v240 offset:34640\n\t"
        "s_waitcnt vmcnt(5)\n\t"
        "s_barrier\n\t"
        "s_waitcnt lgkmcnt(6)\n\t"
        "v_mfma_f32_16x16x32_bf16 v[76:79], v[192:195], v[140:143], v[76:79]\n\t"
        "v_mfma_f32_16x16x32_bf16 v[80:83], v[196:199], v[140:143], v[80:83]\n\t"
        "v_mfma_f32_16x16x32_bf16 v[84:87], v[200:203], v[140:143], v[84:87]\n\t"
        "v_mfma_f32_16x16x32_bf16 v[64:67], v[204:207], v[140:143], v[64:67]\n\t"
        "v_mfma_f32_16x16x32_bf16 v[68:71], v[208:211], v[140:143], v[68:71]\n\t"
        "v_mfma_f32_16x16x32_bf16 v[72:75], v[212:215], v[140:143], v[72:75]\n\t"
        "s_waitcnt lgkmcnt(4)\n\t"
        "v_mfma_f32_16x16x32_bf16 v[76:79], v[204:207], v[144:147], v[76:79]\n\t"
        "ds_read_b64 v[132:133], %[xa0] offset:41568\n\t"
        "ds_read_b64 v[134:135], v240 offset:41568\n\t"
        "v_mfma_f32_16x16x32_bf16 v[80:83], v[208:211], v[144:147], v[80:83]\n\t"
        "v_mfma_f32_16x16x32_bf16 v[84:87], v[212:215], v[144:147], v[84:87]\n\t"
        "s_waitcnt lgkmcnt(4)\n\t"
        "v_mfma_f32_16x16x32_bf16 v[88:91], a[0:3], v[124:127], 0\n\t"
        "ds_read_b64 v[136:137], %[xa0] offset:48496\n\t"
        "ds_read_b64 v[138:139], v240 offset:48496\n\t"
        "v_mfma_f32_16x16x32_bf16 v[92:95], a[4:7], v[124:127], 0\n\t"
        "v_mfma_f32_16x16x32_bf16 v[96:99], a[8:11], v[124:127], 0\n\t"
        "s_waitcnt lgkmcnt(4)\n\t"
        "v_mfma_f32_16x16x32_bf16 v[100:103], a[0:3], v[128:131], 0\n\t"
        "ds_read_b64 v[140:141], %[xa0] offset:27776\n\t"
        "ds_read_b64 v[142:143], v240 offset:27776\n\t"
        "v_mfma_f32_16x16x32_bf16 v[104:107], a[4:7], v[128:131], 0\n\t"
        "v_cvt_pk_bf16_f32 v216, v64, v65\n\t"
        "v_cvt_pk_bf16_f32 v217, v66, v67\n\t"
        "v_mfma_f32_16x16x32_bf16 v[108:111], a[8:11], v[128:131], 0\n\t"
        "v_cvt_pk_bf16_f32 v218, v68, v69\n\t"
        "v_cvt_pk_bf16_f32 v219, v70, v71\n\t"
        "v_mfma_f32_16x16x32_bf16 v[88:91], a[12:15], v[128:131], v[88:91]\n\t"
        "s_nop 0\n\t"
        "v_mfma_f32_16x16x32_bf16 v[92:95], a[16:19], v[128:131], v[92:95]\n\t"
        "v_permlane16_swap_b32 v216, v218\n\t"
        "v_permlane16_swap_b32 v217, v219\n\t"
        "v_mfma_f32_16x16x32_bf16 v[96:99], a[20:23], v[128:131], v[96:99]\n\t"
        "ds_write_b128 %[sw1], v[216:219] offset:13312\n\t"
        "s_waitcnt lgkmcnt(5)\n\t"
        "v_mfma_f32_16x16x32_bf16 v[100:103], a[12:15], v[132:135], v[100:103]\n\t"
        "ds_read_b64 v[144:145], %[xa0] offset:34704\n\t"
        "ds_read_b64 v[146:147], v240 offset:34704\n\t"
        "v_mfma_f32_16x16x32_bf16 v[104:107], a[16:19], v[132:135], v[104:107]\n\t"
        "v_cvt_pk_bf16_f32 v220, v76, v77\n\t"
        "v_cvt_pk_bf16_f32 v221, v78, v79\n\t"
        "v_mfma_f32_16x16x32_bf16 v[108:111], a[20:23], v[132:135], v[108:111]\n\t"
        "v_cvt_pk_bf16_f32 v222, v80, v81\n\t"
        "v_cvt_pk_bf16_f32 v223, v82, v83\n\t"
        "v_mfma_f32_16x16x32_bf16 v[88:91], a[24:27], v[132:135], v[88:91]\n\t"
        "s_nop 0\n\t"
        "v_mfma_f32_16x16x32_bf16 v[92:95], a[28:31], v[132:135], v[92:95]\n\t"
        "v_permlane16_swap_b32 v220, v222\n\t"
        "v_permlane16_swap_b32 v221, v223\n\t"
        "v_mfma_f32_16x16x32_bf16 v[96:99], a[32:35], v[132:135], v[96:99]\n\t"
        "ds_write_b128 %[sw1], v[220:223] offset:19968\n\t"
        "s_waitcnt lgkmcnt(6)\n\t"
        "v_mfma_f32_16x16x32_bf16 v[100:103], a[24:27], v[136:139], v[100:103]\n\t"
        "ds_read_b64 v[124:125], %[xa0] offset:41632\n\t"
        "ds_read_b64 v[126:127], v240 offset:41632\n\t"
        "v_mfma_f32_16x16x32_bf16 v[104:107], a[28:31], v[136:139], v[104:107]\n\t"
        "v_cvt_pk_bf16_f32 v224, v72, v73\n\t"
        "v_cvt_pk_bf16_f32 v225, v74, v75\n\t"
        "v_mfma_f32_16x16x32_bf16 v[108:111], a[32:35], v[136:139], v[108:111]\n\t"
        "v_cvt_pk_bf16_f32 v226, v84, v85\n\t"
        "v_cvt_pk_bf16_f32 v227, v86, v87\n\t"
        "s_waitcnt lgkmcnt(6)\n\t"
        "v_mfma_f32_16x16x32_bf16 v[88:91], a[36:39], v[140:143], v[88:91]\n\t"
        "ds_read_b64 v[128:129], %[xa0] offset:48560\n\t"
        "ds_read_b64 v[130:131], v240 offset:48560\n\t"
        "v_mfma_f32_16x16x32_bf16 v[92:95], a[40:43], v[140:143], v[92:95]\n\t"
        "s_nop 0\n\t"
        "v_mfma_f32_16x16x32_bf16 v[96:99], a[44:47], v[140:143], v[96:99]\n\t"
        "v_permlane16_swap_b32 v224, v226\n\t"
        "v_permlane16_swap_b32 v225, v227\n\t"
        "s_waitcnt lgkmcnt(5)\n\t"
        "v_mfma_f32_16x16x32_bf16 v[100:103], a[36:39], v[144:147], v[100:103]\n\t"
        "ds_read_b64 v[132:133], %[xa0] offset:27840\n\t"
        "ds_read_b64 v[134:135], v240 offset:27840\n\t"
        "v_mfma_f32_16x16x32_bf16 v[104:107], a[40:43], v[144:147], v[104:107]\n\t"
        "ds_write_b128 %[sw2], v[224:227] offset:13312\n\t"
        "v_mfma_f32_16x16x32_bf16 v[108:111], a[44:47], v[144:147], v[108:111]\n\t"
        "v_mfma_f32_16x16x32_bf16 v[88:91], a[48:51], v[144:147], v[88:91]\n\t"
        "s_waitcnt lgkmcnt(0)\n\t"
        "s_barrier\n\t"
        "v_mfma_f32_16x16x32_bf16 v[92:95], a[52:55], v[144:147], v[92:95]\n\t"
        "ds_read_b128 v[228:231], %[rl0] offset:13312\n\t"
        "v_mfma_f32_16x16x32_bf16 v[96:99], a[56:59], v[144:147], v[96:99]\n\t"
        "ds_read_b128 v[232:235], %[rl1] offset:13312\n\t"
        "v_mfma_f32_16x16x32_bf16 v[100:103], a[48:51], v[124:127], v[100:103]\n\t"
        "ds_read_b64 v[136:137], %[xa0] offset:34768\n\t"
        "ds_read_b64 v[138:139], v240 offset:34768\n\t"
        "v_mfma_f32_16x16x32_bf16 v[104:107], a[52:55], v[124:127], v[104:107]\n\t"
        "ds_read_b128 v[236:239], %[rl2] offset:13312\n\t"
        "v_mfma_f32_16x16x32_bf16 v[108:111], a[56:59], v[124:127], v[108:111]\n\t"
        "s_add_u32 s62, %[ob], 172032\n\t"
        "v_mfma_f32_16x16x32_bf16 v[88:91], a[60:63], v[124:127], v[88:91]\n\t"
        "v_mfma_f32_16x16x32_bf16 v[92:95], a[64:67], v[124:127], v[92:95]\n\t"
        "v_mfma_f32_16x16x32_bf16 v[96:99], a[68:71], v[124:127], v[96:99]\n\t"
        "s_waitcnt lgkmcnt(4)\n\t"
        "buffer_store_dwordx4 v[228:231], %[go0], %[orr], s62 offen sc1\n\t"
        "v_mfma_f32_16x16x32_bf16 v[100:103], a[60:63], v[128:131], v[100:103]\n\t"
        "ds_read_b64 v[140:141], %[xa0] offset:41696\n\t"
        "ds_read_b64 v[142:143], v240 offset:41696\n\t"
        "v_mfma_f32_16x16x32_bf16 v[104:107], a[64:67], v[128:131], v[104:107]\n\t"
        "s_waitcnt lgkmcnt(5)\n\t"
        "buffer_store_dwordx4 v[232:235], %[go1], %[orr], s62 offen sc1\n\t"
        "v_mfma_f32_16x16x32_bf16 v[108:111], a[68:71], v[128:131], v[108:111]\n\t"
        "s_waitcnt lgkmcnt(2)\n\t"
        "buffer_store_dwordx4 v[236:239], %[go2], %[orr], s62 offen sc1\n\t"
        "v_mfma_f32_16x16x32_bf16 v[88:91], a[72:75], v[132:135], v[88:91]\n\t"
        "ds_read_b64 v[144:145], %[xa0] offset:48624\n\t"
        "ds_read_b64 v[146:147], v240 offset:48624\n\t"
        "v_mfma_f32_16x16x32_bf16 v[92:95], a[76:79], v[132:135], v[92:95]\n\t"
        "v_mfma_f32_16x16x32_bf16 v[96:99], a[80:83], v[132:135], v[96:99]\n\t"
        "v_mfma_f32_16x16x32_bf16 v[100:103], a[72:75], v[136:139], v[100:103]\n\t"
        "ds_read_b64 v[124:125], %[xa1] offset:27712\n\t"
        "ds_read_b64 v[126:127], v241 offset:27712\n\t"
        "v_mfma_f32_16x16x32_bf16 v[104:107], a[76:79], v[136:139], v[104:107]\n\t"
        "v_mfma_f32_16x16x32_bf16 v[108:111], a[80:83], v[136:139], v[108:111]\n\t"
        "v_mfma_f32_16x16x32_bf16 v[88:91], a[84:87], v[136:139], v[88:91]\n\t"
        "v_mfma_f32_16x16x32_bf16 v[92:95], a[88:91], v[136:139], v[92:95]\n\t"
        "v_mfma_f32_16x16x32_bf16 v[96:99], a[92:95], v[136:139], v[96:99]\n\t"
        "s_waitcnt lgkmcnt(4)\n\t"
        "v_mfma_f32_16x16x32_bf16 v[100:103], a[84:87], v[140:143], v[100:103]\n\t"
        "ds_read_b64 v[128:129], %[xa1] offset:34640\n\t"
        "ds_read_b64 v[130:131], v241 offset:34640\n\t"
        "v_mfma_f32_16x16x32_bf16 v[104:107], a[88:91], v[140:143], v[104:107]\n\t"
        "v_mfma_f32_16x16x32_bf16 v[108:111], a[92:95], v[140:143], v[108:111]\n\t"
        "v_mfma_f32_16x16x32_bf16 v[88:91], a[96:99], v[140:143], v[88:91]\n\t"
        "v_mfma_f32_16x16x32_bf16 v[92:95], a[100:103], v[140:143], v[92:95]\n\t"
        "v_mfma_f32_16x16x32_bf16 v[96:99], a[104:107], v[140:143], v[96:99]\n\t"
        "s_waitcnt lgkmcnt(4)\n\t"
        "v_mfma_f32_16x16x32_bf16 v[100:103], a[96:99], v[144:147], v[100:103]\n\t"
        "ds_read_b64 v[132:133], %[xa1] offset:41568\n\t"
        "ds_read_b64 v[134:135], v241 offset:41568\n\t"
        "v_mfma_f32_16x16x32_bf16 v[104:107], a[100:103], v[144:147], v[104:107]\n\t"
        "v_mfma_f32_16x16x32_bf16 v[108:111], a[104:107], v[144:147], v[108:111]\n\t"
        "s_waitcnt lgkmcnt(4)\n\t"
        "v_mfma_f32_16x16x32_bf16 v[88:91], a[108:111], v[124:127], v[88:91]\n\t"
        "ds_read_b64 v[136:137], %[xa1] offset:48496\n\t"
        "ds_read_b64 v[138:139], v241 offset:48496\n\t"
        "v_mfma_f32_16x16x32_bf16 v[92:95], a[112:115], v[124:127], v[92:95]\n\t"
        "v_mfma_f32_16x16x32_bf16 v[96:99], a[116:119], v[124:127], v[96:99]\n\t"
        "s_waitcnt lgkmcnt(4)\n\t"
        "v_mfma_f32_16x16x32_bf16 v[100:103], a[108:111], v[128:131], v[100:103]\n\t"
        "ds_read_b64 v[140:141], %[xa1] offset:27776\n\t"
        "ds_read_b64 v[142:143], v241 offset:27776\n\t"
        "v_mfma_f32_16x16x32_bf16 v[104:107], a[112:115], v[128:131], v[104:107]\n\t"
        "v_mfma_f32_16x16x32_bf16 v[108:111], a[116:119], v[128:131], v[108:111]\n\t"
        "v_mfma_f32_16x16x32_bf16 v[88:91], a[120:123], v[128:131], v[88:91]\n\t"
        "v_mfma_f32_16x16x32_bf16 v[92:95], a[124:127], v[128:131], v[92:95]\n\t"
        "v_mfma_f32_16x16x32_bf16 v[96:99], a[128:131], v[128:131], v[96:99]\n\t"
        "s_waitcnt lgkmcnt(4)\n\t"
        "v_mfma_f32_16x16x32_bf16 v[100:103], a[120:123], v[132:135], v[100:103]\n\t"
        "ds_read_b64 v[144:145], %[xa1] offset:34704\n\t"
        "ds_read_b64 v[146:147], v241 offset:34704\n\t"
        "v_mfma_f32_16x16x32_bf16 v[104:107], a[124:127], v[132:135], v[104:107]\n\t"
        "v_mfma_f32_16x16x32_bf16 v[108:111], a[128:131], v[132:135], v[108:111]\n\t"
        "v_mfma_f32_16x16x32_bf16 v[88:91], a[132:135], v[132:135], v[88:91]\n\t"
        "v_mfma_f32_16x16x32_bf16 v[92:95], a[136:139], v[132:135], v[92:95]\n\t"
        "v_mfma_f32_16x16x32_bf16 v[96:99], a[140:143], v[132:135], v[96:99]\n\t"
        "s_waitcnt lgkmcnt(4)\n\t"
        "v_mfma_f32_16x16x32_bf16 v[100:103], a[132:135], v[136:139], v[100:103]\n\t"
        "ds_read_b64 v[124:125], %[xa1] offset:41632\n\t"
        "ds_read_b64 v[126:127], v241 offset:41632\n\t"
        "v_mfma_f32_16x16x32_bf16 v[104:107], a[136:139], v[136:139], v[104:107]\n\t"
        "v_mfma_f32_16x16x32_bf16 v[108:111], a[140:143], v[136:139], v[108:111]\n\t"
        "s_waitcnt lgkmcnt(4)\n\t"
        "v_mfma_f32_16x16x32_bf16 v[88:91], a[144:147], v[140:143], v[88:91]\n\t"
        "ds_read_b64 v[128:129], %[xa1] offset:48560\n\t"
        "ds_read_b64 v[130:131], v241 offset:48560\n\t"
        "v_mfma_f32_16x16x32_bf16 v[92:95], a[148:151], v[140:143], v[92:95]\n\t"
        "v_mfma_f32_16x16x32_bf16 v[96:99], a[152:155], v[140:143], v[96:99]\n\t"
        "s_waitcnt lgkmcnt(4)\n\t"
        "v_mfma_f32_16x16x32_bf16 v[100:103], a[144:147], v[144:147], v[100:103]\n\t"
        "ds_read_b64 v[132:133], %[xa1] offset:27840\n\t"
        "ds_read_b64 v[134:135], v241 offset:27840\n\t"
        "v_mfma_f32_16x16x32_bf16 v[104:107], a[148:151], v[144:147], v[104:107]\n\t"
        "v_mfma_f32_16x16x32_bf16 v[108:111], a[152:155], v[144:147], v[108:111]\n\t"
        "v_mfma_f32_16x16x32_bf16 v[88:91], a[156:159], v[144:147], v[88:91]\n\t"
        "v_mfma_f32_16x16x32_bf16 v[92:95], a[160:163], v[144:147], v[92:95]\n\t"
        "v_mfma_f32_16x16x32_bf16 v[96:99], a[164:167], v[144:147], v[96:99]\n\t"
        "s_waitcnt lgkmcnt(4)\n\t"
        "v_mfma_f32_16x16x32_bf16 v[100:103], a[156:159], v[124:127], v[100:103]\n\t"
        "ds_read_b64 v[136:137], %[xa1] offset:34768\n\t"
        "ds_read_b64 v[138:139], v241 offset:34768\n\t"
        "v_mfma_f32_16x16x32_bf16 v[104:107], a[160:163], v[124:127], v[104:107]\n\t"
        "v_mfma_f32_16x16x32_bf16 v[108:111], a[164:167], v[124:127], v[108:111]\n\t"
        "v_mfma_f32_16x16x32_bf16 v[88:91], a[168:171], v[124:127], v[88:91]\n\t"
        "v_mfma_f32_16x16x32_bf16 v[92:95], a[172:175], v[124:127], v[92:95]\n\t"
        "v_mfma_f32_16x16x32_bf16 v[96:99], a[176:179], v[124:127], v[96:99]\n\t"
        "s_waitcnt lgkmcnt(4)\n\t"
        "v_mfma_f32_16x16x32_bf16 v[100:103], a[168:171], v[128:131], v[100:103]\n\t"
        "ds_read_b64 v[140:141], %[xa1] offset:41696\n\t"
        "ds_read_b64 v[142:143], v241 offset:41696\n\t"
        "v_mfma_f32_16x16x32_bf16 v[104:107], a[172:175], v[128:131], v[104:107]\n\t"
        "v_mfma_f32_16x16x32_bf16 v[108:111], a[176:179], v[128:131], v[108:111]\n\t"
        "s_waitcnt lgkmcnt(4)\n\t"
        "v_mfma_f32_16x16x32_bf16 v[88:91], a[180:183], v[132:135], v[88:91]\n\t"
        "ds_read_b64 v[144:145], %[xa1] offset:48624\n\t"
        "ds_read_b64 v[146:147], v241 offset:48624\n\t"
        "v_mfma_f32_16x16x32_bf16 v[92:95], a[184:187], v[132:135], v[92:95]\n\t"
        "v_mfma_f32_16x16x32_bf16 v[96:99], a[188:191], v[132:135], v[96:99]\n\t"
        "s_waitcnt lgkmcnt(4)\n\t"
        "v_mfma_f32_16x16x32_bf16 v[100:103], a[180:183], v[136:139], v[100:103]\n\t"
        "ds_read_b64 v[124:125], %[xa2] offset:27712\n\t"
        "ds_read_b64 v[126:127], v242 offset:27712\n\t"
        "v_mfma_f32_16x16x32_bf16 v[104:107], a[184:187], v[136:139], v[104:107]\n\t"
        "v_mfma_f32_16x16x32_bf16 v[108:111], a[188:191], v[136:139], v[108:111]\n\t"
        "v_mfma_f32_16x16x32_bf16 v[88:91], a[192:195], v[136:139], v[88:91]\n\t"
        "v_mfma_f32_16x16x32_bf16 v[92:95], a[196:199], v[136:139], v[92:95]\n\t"
        "v_mfma_f32_16x16x32_bf16 v[96:99], a[200:203], v[136:139], v[96:99]\n\t"
        "s_waitcnt lgkmcnt(4)\n\t"
        "v_mfma_f32_16x16x32_bf16 v[100:103], a[192:195], v[140:143], v[100:103]\n\t"
        "ds_read_b64 v[128:129], %[xa2] offset:34640\n\t"
        "ds_read_b64 v[130:131], v242 offset:34640\n\t"
        "v_mfma_f32_16x16x32_bf16 v[104:107], a[196:199], v[140:143], v[104:107]\n\t"
        "v_mfma_f32_16x16x32_bf16 v[108:111], a[200:203], v[140:143], v[108:111]\n\t"
        "v_mfma_f32_16x16x32_bf16 v[88:91], a[204:207], v[140:143], v[88:91]\n\t"
        "v_mfma_f32_16x16x32_bf16 v[92:95], a[208:211], v[140:143], v[92:95]\n\t"
        "v_mfma_f32_16x16x32_bf16 v[96:99], a[212:215], v[140:143], v[96:99]\n\t"
        "s_waitcnt lgkmcnt(4)\n\t"
        "v_mfma_f32_16x16x32_bf16 v[100:103], a[204:207], v[144:147], v[100:103]\n\t"
        "ds_read_b64 v[132:133], %[xa2] offset:41568\n\t"
        "ds_read_b64 v[134:135], v242 offset:41568\n\t"
        "v_mfma_f32_16x16x32_bf16 v[104:107], a[208:211], v[144:147], v[104:107]\n\t"
        "v_mfma_f32_16x16x32_bf16 v[108:111], a[212:215], v[144:147], v[108:111]\n\t"
        "s_waitcnt lgkmcnt(4)\n\t"
        "v_mfma_f32_16x16x32_bf16 v[88:91], a[216:219], v[124:127], v[88:91]\n\t"
        "ds_read_b64 v[136:137], %[xa2] offset:48496\n\t"
        "ds_read_b64 v[138:139], v242 offset:48496\n\t"
        "v_mfma_f32_16x16x32_bf16 v[92:95], a[220:223], v[124:127], v[92:95]\n\t"
        "v_mfma_f32_16x16x32_bf16 v[96:99], a[224:227], v[124:127], v[96:99]\n\t"
        "s_waitcnt lgkmcnt(4)\n\t"
        "v_mfma_f32_16x16x32_bf16 v[100:103], a[216:219], v[128:131], v[100:103]\n\t"
        "ds_read_b64 v[140:141], %[xa2] offset:27776\n\t"
        "ds_read_b64 v[142:143], v242 offset:27776\n\t"
        "v_mfma_f32_16x16x32_bf16 v[104:107], a[220:223], v[128:131], v[104:107]\n\t"
        "v_mfma_f32_16x16x32_bf16 v[108:111], a[224:227], v[128:131], v[108:111]\n\t"
        "v_mfma_f32_16x16x32_bf16 v[88:91], a[228:231], v[128:131], v[88:91]\n\t"
        "v_mfma_f32_16x16x32_bf16 v[92:95], a[232:235], v[128:131], v[92:95]\n\t"
        "v_mfma_f32_16x16x32_bf16 v[96:99], a[236:239], v[128:131], v[96:99]\n\t"
        "s_waitcnt lgkmcnt(4)\n\t"
        "v_mfma_f32_16x16x32_bf16 v[100:103], a[228:231], v[132:135], v[100:103]\n\t"
        "ds_read_b64 v[144:145], %[xa2] offset:34704\n\t"
        "ds_read_b64 v[146:147], v242 offset:34704\n\t"
        "v_mfma_f32_16x16x32_bf16 v[104:107], a[232:235], v[132:135], v[104:107]\n\t"
        "v_mfma_f32_16x16x32_bf16 v[108:111], a[236:239], v[132:135], v[108:111]\n\t"
        "v_mfma_f32_16x16x32_bf16 v[88:91], a[240:243], v[132:135], v[88:91]\n\t"
        "v_mfma_f32_16x16x32_bf16 v[92:95], a[244:247], v[132:135], v[92:95]\n\t"
        "v_mfma_f32_16x16x32_bf16 v[96:99], a[248:251], v[132:135], v[96:99]\n\t"
        "s_waitcnt lgkmcnt(4)\n\t"
        "v_mfma_f32_16x16x32_bf16 v[100:103], a[240:243], v[136:139], v[100:103]\n\t"
        "ds_read_b64 v[124:125], %[xa2] offset:41632\n\t"
        "ds_read_b64 v[126:127], v242 offset:41632\n\t"
        "v_mfma_f32_16x16x32_bf16 v[104:107], a[244:247], v[136:139], v[104:107]\n\t"
        "v_mfma_f32_16x16x32_bf16 v[108:111], a[248:251], v[136:139], v[108:111]\n\t"
        "s_waitcnt lgkmcnt(4)\n\t"
        "v_mfma_f32_16x16x32_bf16 v[88:91], a[252:255], v[140:143], v[88:91]\n\t"
        "ds_read_b64 v[128:129], %[xa2] offset:48560\n\t"
        "ds_read_b64 v[130:131], v242 offset:48560\n\t"
        "v_mfma_f32_16x16x32_bf16 v[92:95], v[148:151], v[140:143], v[92:95]\n\t"
        "v_mfma_f32_16x16x32_bf16 v[96:99], v[152:155], v[140:143], v[96:99]\n\t"
        "s_waitcnt lgkmcnt(4)\n\t"
        "v_mfma_f32_16x16x32_bf16 v[100:103], a[252:255], v[144:147], v[100:103]\n\t"
        "ds_read_b64 v[132:133], %[xa2] offset:27840\n\t"
        "ds_read_b64 v[134:135], v242 offset:27840\n\t"
        "v_mfma_f32_16x16x32_bf16 v[104:107], v[148:151], v[144:147], v[104:107]\n\t"
        "v_mfma_f32_16x16x32_bf16 v[108:111], v[152:155], v[144:147], v[108:111]\n\t"
        "v_mfma_f32_16x16x32_bf16 v[88:91], v[156:159], v[144:147], v[88:91]\n\t"
        "v_mfma_f32_16x16x32_bf16 v[92:95], v[160:163], v[144:147], v[92:95]\n\t"
        "v_mfma_f32_16x16x32_bf16 v[96:99], v[164:167], v[144:147], v[96:99]\n\t"
        "s_waitcnt lgkmcnt(4)\n\t"
        "v_mfma_f32_16x16x32_bf16 v[100:103], v[156:159], v[124:127], v[100:103]\n\t"
        "ds_read_b64 v[136:137], %[xa2] offset:34768\n\t"
        "ds_read_b64 v[138:139], v242 offset:34768\n\t"
        "v_mfma_f32_16x16x32_bf16 v[104:107], v[160:163], v[124:127], v[104:107]\n\t"
        "v_mfma_f32_16x16x32_bf16 v[108:111], v[164:167], v[124:127], v[108:111]\n\t"
        "v_mfma_f32_16x16x32_bf16 v[88:91], v[168:171], v[124:127], v[88:91]\n\t"
        "v_mfma_f32_16x16x32_bf16 v[92:95], v[172:175], v[124:127], v[92:95]\n\t"
        "v_mfma_f32_16x16x32_bf16 v[96:99], v[176:179], v[124:127], v[96:99]\n\t"
        "s_waitcnt lgkmcnt(4)\n\t"
        "v_mfma_f32_16x16x32_bf16 v[100:103], v[168:171], v[128:131], v[100:103]\n\t"
        "ds_read_b64 v[140:141], %[xa2] offset:41696\n\t"
        "ds_read_b64 v[142:143], v242 offset:41696\n\t"
        "v_mfma_f32_16x16x32_bf16 v[104:107], v[172:175], v[128:131], v[104:107]\n\t"
        "v_mfma_f32_16x16x32_bf16 v[108:111], v[176:179], v[128:131], v[108:111]\n\t"
        "s_waitcnt lgkmcnt(4)\n\t"
        "v_mfma_f32_16x16x32_bf16 v[88:91], v[180:183], v[132:135], v[88:91]\n\t"
        "ds_read_b64 v[144:145], %[xa2] offset:48624\n\t"
        "ds_read_b64 v[146:147], v242 offset:48624\n\t"
        "v_mfma_f32_16x16x32_bf16 v[92:95], v[184:187], v[132:135], v[92:95]\n\t"
        "v_mfma_f32_16x16x32_bf16 v[96:99], v[188:191], v[132:135], v[96:99]\n\t"
        "s_waitcnt lgkmcnt(4)\n\t"
        "v_mfma_f32_16x16x32_bf16 v[100:103], v[180:183], v[136:139], v[100:103]\n\t"
        "ds_read_b64 v[124:125], %[xa0] offset:41568\n\t"
        "ds_read_b64 v[126:127], v240 offset:41568\n\t"
        "v_mfma_f32_16x16x32_bf16 v[104:107], v[184:187], v[136:139], v[104:107]\n\t"
        "v_mfma_f32_16x16x32_bf16 v[108:111], v[188:191], v[136:139], v[108:111]\n\t"
        "v_mfma_f32_16x16x32_bf16 v[88:91], v[192:195], v[136:139], v[88:91]\n\t"
        "v_mfma_f32_16x16x32_bf16 v[92:95], v[196:199], v[136:139], v[92:95]\n\t"
        "v_mfma_f32_16x16x32_bf16 v[96:99], v[200:203], v[136:139], v[96:99]\n\t"
        "ds_read_b64 v[128:129], %[xa0] offset:48496\n\t"
        "ds_read_b64 v[130:131], v240 offset:48496\n\t"
        "s_waitcnt vmcnt(6)\n\t"
        "s_barrier\n\t"
        "s_waitcnt lgkmcnt(6)\n\t"
        "v_mfma_f32_16x16x32_bf16 v[100:103], v[192:195], v[140:143], v[100:103]\n\t"
        "v_mfma_f32_16x16x32_bf16 v[104:107], v[196:199], v[140:143], v[104:107]\n\t"
        "v_mfma_f32_16x16x32_bf16 v[108:111], v[200:203], v[140:143], v[108:111]\n\t"
        "v_mfma_f32_16x16x32_bf16 v[88:91], v[204:207], v[140:143], v[88:91]\n\t"
        "v_mfma_f32_16x16x32_bf16 v[92:95], v[208:211], v[140:143], v[92:95]\n\t"
        "v_mfma_f32_16x16x32_bf16 v[96:99], v[212:215], v[140:143], v[96:99]\n\t"
        "s_waitcnt lgkmcnt(4)\n\t"
        "v_mfma_f32_16x16x32_bf16 v[100:103], v[204:207], v[144:147], v[100:103]\n\t"
        "ds_read_b64 v[132:133], %[xa0] offset:55424\n\t"
        "ds_read_b64 v[134:135], v240 offset:55424\n\t"
        "v_mfma_f32_16x16x32_bf16 v[104:107], v[208:211], v[144:147], v[104:107]\n\t"
        "v_mfma_f32_16x16x32_bf16 v[108:111], v[212:215], v[144:147], v[108:111]\n\t"
        "s_waitcnt lgkmcnt(4)\n\t"
        "v_mfma_f32_16x16x32_bf16 v[112:115], a[0:3], v[124:127], 0\n\t"
        "ds_read_b64 v[136:137], %[xa0] offset:41632\n\t"
        "ds_read_b64 v[138:139], v240 offset:41632\n\t"
        "v_mfma_f32_16x16x32_bf16 v[116:119], a[4:7], v[124:127], 0\n\t"
        "v_mfma_f32_16x16x32_bf16 v[120:123], a[8:11], v[124:127], 0\n\t"
        "s_waitcnt lgkmcnt(4)\n\t"
        "v_mfma_f32_16x16x32_bf16 v[112:115], a[12:15], v[128:131], v[112:115]\n\t"
        "ds_read_b64 v[140:141], %[xa0] offset:48560\n\t"
        "ds_read_b64 v[142:143], v240 offset:48560\n\t"
        "v_mfma_f32_16x16x32_bf16 v[116:119], a[16:19], v[128:131], v[116:119]\n\t"
        "v_cvt_pk_bf16_f32 v216, v88, v89\n\t"
        "v_cvt_pk_bf16_f32 v217, v90, v91\n\t"
        "v_mfma_f32_16x16x32_bf16 v[120:123], a[20:23], v[128:131], v[120:123]\n\t"
        "v_cvt_pk_bf16_f32 v218, v92, v93\n\t"
        "v_cvt_pk_bf16_f32 v219, v94, v95\n\t"
        "s_waitcnt lgkmcnt(4)\n\t"
        "v_mfma_f32_16x16x32_bf16 v[112:115], a[24:27], v[132:135], v[112:115]\n\t"
        "ds_read_b64 v[144:145], %[xa0] offset:55488\n\t"
        "ds_read_b64 v[146:147], v240 offset:55488\n\t"
        "v_mfma_f32_16x16x32_bf16 v[116:119], a[28:31], v[132:135], v[116:119]\n\t"
        "s_nop 0\n\t"
        "v_mfma_f32_16x16x32_bf16 v[120:123], a[32:35], v[132:135], v[120:123]\n\t"
        "v_permlane16_swap_b32 v216, v218\n\t"
        "v_permlane16_swap_b32 v217, v219\n\t"
        "s_waitcnt lgkmcnt(4)\n\t"
        "v_mfma_f32_16x16x32_bf16 v[112:115], a[36:39], v[136:139], v[112:115]\n\t"
        "ds_read_b64 v[124:125], %[xa0] offset:41696\n\t"
        "ds_read_b64 v[126:127], v240 offset:41696\n\t"
        "v_mfma_f32_16x16x32_bf16 v[116:119], a[40:43], v[136:139], v[116:119]\n\t"
        "ds_write_b128 %[sw1], v[216:219] offset:26624\n\t"
        "v_mfma_f32_16x16x32_bf16 v[120:123], a[44:47], v[136:139], v[120:123]\n\t"
        "v_cvt_pk_bf16_f32 v220, v100, v101\n\t"
        "v_cvt_pk_bf16_f32 v221, v102, v103\n\t"
        "s_waitcnt lgkmcnt(5)\n\t"
        "v_mfma_f32_16x16x32_bf16 v[112:115], a[48:51], v[140:143], v[112:115]\n\t"
        "ds_read_b64 v[128:129], %[xa0] offset:48624\n\t"
        "ds_read_b64 v[130:131], v240 offset:48624\n\t"
        "v_mfma_f32_16x16x32_bf16 v[116:119], a[52:55], v[140:143], v[116:119]\n\t"
        "v_cvt_pk_bf16_f32 v222, v104, v105\n\t"
        "v_cvt_pk_bf16_f32 v223, v106, v107\n\t"
        "v_mfma_f32_16x16x32_bf16 v[120:123], a[56:59], v[140:143], v[120:123]\n\t"
        "s_nop 0\n\t"
        "s_waitcnt lgkmcnt(5)\n\t"
        "v_mfma_f32_16x16x32_bf16 v[112:115], a[60:63], v[144:147], v[112:115]\n\t"
        "ds_read_b64 v[132:133], %[xa0] offset:55552\n\t"
        "ds_read_b64 v[134:135], v240 offset:55552\n\t"
        "v_mfma_f32_16x16x32_bf16 v[116:119], a[64:67], v[144:147], v[116:119]\n\t"
        "v_permlane16_swap_b32 v220, v222\n\t"
        "v_permlane16_swap_b32 v221, v223\n\t"
        "v_mfma_f32_16x16x32_bf16 v[120:123], a[68:71], v[144:147], v[120:123]\n\t"
        "ds_write_b128 %[sw1], v[220:223] offset:33280\n\t"
        "s_waitcnt lgkmcnt(6)\n\t"
        "v_mfma_f32_16x16x32_bf16 v[112:115], a[72:75], v[124:127], v[112:115]\n\t"
        "ds_read_b64 v[136:137], %[xa1] offset:41568\n\t"
        "ds_read_b64 v[138:139], v241 offset:41568\n\t"
        "v_mfma_f32_16x16x32_bf16 v[116:119], a[76:79], v[124:127], v[116:119]\n\t"
        "v_cvt_pk_bf16_f32 v224, v96, v97\n\t"
        "v_cvt_pk_bf16_f32 v225, v98, v99\n\t"
        "v_mfma_f32_16x16x32_bf16 v[120:123], a[80:83], v[124:127], v[120:123]\n\t"
        "v_cvt_pk_bf16_f32 v226, v108, v109\n\t"
        "v_cvt_pk_bf16_f32 v227, v110, v111\n\t"
        "s_waitcnt lgkmcnt(5)\n\t"
        "v_mfma_f32_16x16x32_bf16 v[112:115], a[84:87], v[128:131], v[112:115]\n\t"
        "ds_read_b64 v[140:141], %[xa1] offset:48496\n\t"
        "ds_read_b64 v[142:143], v241 offset:48496\n\t"
        "v_mfma_f32_16x16x32_bf16 v[116:119], a[88:91], v[128:131], v[116:119]\n\t"
        "s_nop 0\n\t"
        "v_mfma_f32_16x16x32_bf16 v[120:123], a[92:95], v[128:131], v[120:123]\n\t"
        "v_permlane16_swap_b32 v224, v226\n\t"
        "v_permlane16_swap_b32 v225, v227\n\t"
        "s_waitcnt lgkmcnt(5)\n\t"
        "v_mfma_f32_16x16x32_bf16 v[112:115], a[96:99], v[132:135], v[112:115]\n\t"
        "ds_read_b64 v[144:145], %[xa1] offset:55424\n\t"
        "ds_read_b64 v[146:147], v241 offset:55424\n\t"
        "v_mfma_f32_16x16x32_bf16 v[116:119], a[100:103], v[132:135], v[116:119]\n\t"
        "ds_write_b128 %[sw2], v[224:227] offset:26624\n\t"
        "v_mfma_f32_16x16x32_bf16 v[120:123], a[104:107], v[132:135], v[120:123]\n\t"
        "s_waitcnt lgkmcnt(5)\n\t"
        "v_mfma_f32_16x16x32_bf16 v[112:115], a[108:111], v[136:139], v[112:115]\n\t"
        "ds_read_b64 v[124:125], %[xa1] offset:41632\n\t"
        "ds_read_b64 v[126:127], v241 offset:41632\n\t"
        "v_mfma_f32_16x16x32_bf16 v[116:119], a[112:115], v[136:139], v[116:119]\n\t"
        "s_waitcnt lgkmcnt(2)\n\t"
        "s_barrier\n\t"
        "v_mfma_f32_16x16x32_bf16 v[120:123], a[116:119], v[136:139], v[120:123]\n\t"
        "ds_read_b128 v[228:231], %[rl0] offset:26624\n\t"
        "v_mfma_f32_16x16x32_bf16 v[112:115], a[120:123], v[140:143], v[112:115]\n\t"
        "ds_read_b64 v[128:129], %[xa1] offset:48560\n\t"
        "ds_read_b64 v[130:131], v241 offset:48560\n\t"
        "v_mfma_f32_16x16x32_bf16 v[116:119], a[124:127], v[140:143], v[116:119]\n\t"
        "ds_read_b128 v[232:235], %[rl1] offset:26624\n\t"
        "v_mfma_f32_16x16x32_bf16 v[120:123], a[128:131], v[140:143], v[120:123]\n\t"
        "ds_read_b128 v[236:239], %[rl2] offset:26624\n\t"
        "v_mfma_f32_16x16x32_bf16 v[112:115], a[132:135], v[144:147], v[112:115]\n\t"
        "ds_read_b64 v[132:133], %[xa1] offset:55488\n\t"
        "ds_read_b64 v[134:135], v241 offset:55488\n\t"
        "v_mfma_f32_16x16x32_bf16 v[116:119], a[136:139], v[144:147], v[116:119]\n\t"
        "s_add_u32 s62, %[ob], 344064\n\t"
        "v_mfma_f32_16x16x32_bf16 v[120:123], a[140:143], v[144:147], v[120:123]\n\t"
        "s_waitcnt lgkmcnt(7)\n\t"
        "v_mfma_f32_16x16x32_bf16 v[112:115], a[144:147], v[124:127], v[112:115]\n\t"
        "ds_read_b64 v[136:137], %[xa1] offset:41696\n\t"
        "ds_read_b64 v[138:139], v241 offset:41696\n\t"
        "v_mfma_f32_16x16x32_bf16 v[116:119], a[148:151], v[124:127], v[116:119]\n\t"
        "v_mfma_f32_16x16x32_bf16 v[120:123], a[152:155], v[124:127], v[120:123]\n\t"
        "s_waitcnt lgkmcnt(8)\n\t"
        "buffer_store_dwordx4 v[228:231], %[go0], %[orr], s62 offen sc1\n\t"
        "s_waitcnt lgkmcnt(6)\n\t"
        "v_mfma_f32_16x16x32_bf16 v[112:115], a[156:159], v[128:131], v[112:115]\n\t"
        "ds_read_b64 v[140:141], %[xa1] offset:48624\n\t"
        "ds_read_b64 v[142:143], v241 offset:48624\n\t"
        "v_mfma_f32_16x16x32_bf16 v[116:119], a[160:163], v[128:131], v[116:119]\n\t"
        "s_waitcnt lgkmcnt(7)\n\t"
        "buffer_store_dwordx4 v[232:235], %[go1], %[orr], s62 offen sc1\n\t"
        "v_mfma_f32_16x16x32_bf16 v[120:123], a[164:167], v[128:131], v[120:123]\n\t"
        "s_waitcnt lgkmcnt(6)\n\t"
        "buffer_store_dwordx4 v[236:239], %[go2], %[orr], s62 offen sc1\n\t"
        "s_waitcnt lgkmcnt(4)\n\t"
        "v_mfma_f32_16x16x32_bf16 v[112:115], a[168:171], v[132:135], v[112:115]\n\t"
        "ds_read_b64 v[144:145], %[xa1] offset:55552\n\t"
        "ds_read_b64 v[146:147], v241 offset:55552\n\t"
        "v_mfma_f32_16x16x32_bf16 v[116:119], a[172:175], v[132:135], v[116:119]\n\t"
        "v_mfma_f32_16x16x32_bf16 v[120:123], a[176:179], v[132:135], v[120:123]\n\t"
        "s_waitcnt lgkmcnt(4)\n\t"
        "v_mfma_f32_16x16x32_bf16 v[112:115], a[180:183], v[136:139], v[112:115]\n\t"
        "ds_read_b64 v[124:125], %[xa2] offset:41568\n\t"
        "ds_read_b64 v[126:127], v242 offset:41568\n\t"
        "v_mfma_f32_16x16x32_bf16 v[116:119], a[184:187], v[136:139], v[116:119]\n\t"
        "v_mfma_f32_16x16x32_bf16 v[120:123], a[188:191], v[136:139], v[120:123]\n\t"
        "s_waitcnt lgkmcnt(4)\n\t"
        "v_mfma_f32_16x16x32_bf16 v[112:115], a[192:195], v[140:143], v[112:115]\n\t"
        "ds_read_b64 v[128:129], %[xa2] offset:48496\n\t"
        "ds_read_b64 v[130:131], v242 offset:48496\n\t"
        "v_mfma_f32_16x16x32_bf16 v[116:119], a[196:199], v[140:143], v[116:119]\n\t"
        "v_mfma_f32_16x16x32_bf16 v[120:123], a[200:203], v[140:143], v[120:123]\n\t"
        "s_waitcnt lgkmcnt(4)\n\t"
        "v_mfma_f32_16x16x32_bf16 v[112:115], a[204:207], v[144:147], v[112:115]\n\t"
        "ds_read_b64 v[132:133], %[xa2] offset:55424\n\t"
        "ds_read_b64 v[134:135], v242 offset:55424\n\t"
        "v_mfma_f32_16x16x32_bf16 v[116:119], a[208:211], v[144:147], v[116:119]\n\t"
        "v_mfma_f32_16x16x32_bf16 v[120:123], a[212:215], v[144:147], v[120:123]\n\t"
        "s_waitcnt lgkmcnt(4)\n\t"
        "v_mfma_f32_16x16x32_bf16 v[112:115], a[216:219], v[124:127], v[112:115]\n\t"
        "ds_read_b64 v[136:137], %[xa2] offset:41632\n\t"
        "ds_read_b64 v[138:139], v242 offset:41632\n\t"
        "v_mfma_f32_16x16x32_bf16 v[116:119], a[220:223], v[124:127], v[116:119]\n\t"
        "v_mfma_f32_16x16x32_bf16 v[120:123], a[224:227], v[124:127], v[120:123]\n\t"
        "s_waitcnt lgkmcnt(4)\n\t"
        "v_mfma_f32_16x16x32_bf16 v[112:115], a[228:231], v[128:131], v[112:115]\n\t"
        "ds_read_b64 v[140:141], %[xa2] offset:48560\n\t"
        "ds_read_b64 v[142:143], v242 offset:48560\n\t"
        "v_mfma_f32_16x16x32_bf16 v[116:119], a[232:235], v[128:131], v[116:119]\n\t"
        "v_mfma_f32_16x16x32_bf16 v[120:123], a[236:239], v[128:131], v[120:123]\n\t"
        "s_waitcnt lgkmcnt(4)\n\t"
        "v_mfma_f32_16x16x32_bf16 v[112:115], a[240:243], v[132:135], v[112:115]\n\t"
        "ds_read_b64 v[144:145], %[xa2] offset:55488\n\t"
        "ds_read_b64 v[146:147], v242 offset:55488\n\t"
        "v_mfma_f32_16x16x32_bf16 v[116:119], a[244:247], v[132:135], v[116:119]\n\t"
        "v_mfma_f32_16x16x32_bf16 v[120:123], a[248:251], v[132:135], v[120:123]\n\t"
        "s_waitcnt lgkmcnt(4)\n\t"
        "v_mfma_f32_16x16x32_bf16 v[112:115], a[252:255], v[136:139], v[112:115]\n\t"
        "ds_read_b64 v[124:125], %[xa2] offset:41696\n\t"
        "ds_read_b64 v[126:127], v242 offset:41696\n\t"
        "v_mfma_f32_16x16x32_bf16 v[116:119], v[148:151], v[136:139], v[116:119]\n\t"
        "v_mfma_f32_16x16x32_bf16 v[120:123], v[152:155], v[136:139], v[120:123]\n\t"
        "s_waitcnt lgkmcnt(4)\n\t"
        "v_mfma_f32_16x16x32_bf16 v[112:115], v[156:159], v[140:143], v[112:115]\n\t"
        "ds_read_b64 v[128:129], %[xa2] offset:48624\n\t"
        "ds_read_b64 v[130:131], v242 offset:48624\n\t"
        "v_mfma_f32_16x16x32_bf16 v[116:119], v[160:163], v[140:143], v[116:119]\n\t"
        "v_mfma_f32_16x16x32_bf16 v[120:123], v[164:167], v[140:143], v[120:123]\n\t"
        "s_waitcnt lgkmcnt(4)\n\t"
        "v_mfma_f32_16x16x32_bf16 v[112:115], v[168:171], v[144:147], v[112:115]\n\t"
        "ds_read_b64 v[132:133], %[xa2] offset:55552\n\t"
        "ds_read_b64 v[134:135], v242 offset:55552\n\t"
        "v_mfma_f32_16x16x32_bf16 v[116:119], v[172:175], v[144:147], v[116:119]\n\t"
        "v_mfma_f32_16x16x32_bf16 v[120:123], v[176:179], v[144:147], v[120:123]\n\t"
        "s_waitcnt lgkmcnt(4)\n\t"
        "v_mfma_f32_16x16x32_bf16 v[112:115], v[180:183], v[124:127], v[112:115]\n\t"
        "v_mfma_f32_16x16x32_bf16 v[116:119], v[184:187], v[124:127], v[116:119]\n\t"
        "v_mfma_f32_16x16x32_bf16 v[120:123], v[188:191], v[124:127], v[120:123]\n\t"
        "s_waitcnt lgkmcnt(2)\n\t"
        "v_mfma_f32_16x16x32_bf16 v[112:115], v[192:195], v[128:131], v[112:115]\n\t"
        "v_mfma_f32_16x16x32_bf16 v[116:119], v[196:199], v[128:131], v[116:119]\n\t"
        "v_mfma_f32_16x16x32_bf16 v[120:123], v[200:203], v[128:131], v[120:123]\n\t"
        "s_waitcnt lgkmcnt(0)\n\t"
        "v_mfma_f32_16x16x32_bf16 v[112:115], v[204:207], v[132:135], v[112:115]\n\t"
        "v_mfma_f32_16x16x32_bf16 v[116:119], v[208:211], v[132:135], v[116:119]\n\t"
        "v_mfma_f32_16x16x32_bf16 v[120:123], v[212:215], v[132:135], v[120:123]\n\t"
        "s_nop 7\n\t"
        "s_nop 1\n\t"
        "v_cvt_pk_bf16_f32 v216, v112, v113\n\t"
        "v_cvt_pk_bf16_f32 v217, v114, v115\n\t"
        "s_nop 1\n\t"
        "v_cvt_pk_bf16_f32 v218, v116, v117\n\t"
        "v_cvt_pk_bf16_f32 v219, v118, v119\n\t"
        "s_nop 1\n\t"
        "s_nop 0\n\t"
        "s_nop 1\n\t"
        "v_permlane16_swap_b32 v216, v218\n\t"
        "v_permlane16_swap_b32 v217, v219\n\t"
        "s_nop 1\n\t"
        "ds_write_b128 %[sw1], v[216:219] offset:39936\n\t"
        "s_nop 1\n\t"
        "v_cvt_pk_bf16_f32 v220, v120, v121\n\t"
        "v_cvt_pk_bf16_f32 v221, v122, v123\n\t"
        "s_nop 1\n\t"
        "ds_write_b64 %[sw3], v[220:221] offset:39936\n\t"
        "s_nop 1\n\t"
        "s_nop 1\n\t"
        "s_waitcnt lgkmcnt(0)\n\t"
        "s_barrier\n\t"
        "s_nop 1\n\t"
        "ds_read_b128 v[224:227], %[rl0] offset:39936\n\t"
        "s_nop 1\n\t"
        "ds_read_b128 v[228:231], %[rl1] offset:39936\n\t"
        "s_nop 1\n\t"
        "s_add_u32 s62, %[ob], 516096\n\t"
        "s_nop 1\n\t"
        "s_nop 1\n\t"
        "s_nop 1\n\t"
        "s_waitcnt lgkmcnt(1)\n\t"
        "buffer_store_dwordx4 v[224:227], %[go0], %[orr], s62 offen sc1\n\t"
        "s_nop 1\n\t"
        "s_waitcnt lgkmcnt(0)\n\t"
        "s_mov_b32 exec_hi, 0\n\t"
        "buffer_store_dwordx4 v[228:231], %[go1], %[orr], s62 offen sc1\n\t"
        "s_mov_b32 exec_hi, -1\n\t"
        "s_nop 1\n\t"
        :
        : [xo0] "v"(xo[0]), [xo1] "v"(xo[1]),
          [wo0] "v"(wo[0]), [wo1] "v"(wo[1]), [wo2] "v"(wo[2]), [wo3] "v"(wo[3]), [wo4] "v"(wo[4]),
          [xa0] "v"(xa[0]), [xa1] "v"(xa[1]), [xa2] "v"(xa[2]),
          [wa00] "v"(wa[0][0]), [wa01] "v"(wa[0][1]), [wa10] "v"(wa[1][0]), [wa11] "v"(wa[1][1]),
          [wa20] "v"(wa[2][0]), [wa21] "v"(wa[2][1]),
          [sw1] "v"(sw1), [sw2] "v"(sw2), [sw3] "v"(sw3),
          [rl0] "v"(rl[0]), [rl1] "v"(rl[1]), [rl2] "v"(rl[2]), [go0] "v"(go[0]), [go1] "v"(go[1]), [go2] "v"(go[2]),
          [xr] "s"(xr), [wr] "s"(wr), [orr] "s"(orr),
          [xb] "s"(xb), [ob] "s"(ob), [mx] "s"(mx), [mw] "s"(mw)
        : "v40", "v41", "v42", "v43", "v44", "v45", "v46", "v47", "v48", "v49", "v50", "v51", "v52", "v53", "v54", "v55", "v56", "v57", "v58", "v59", "v60", "v61", "v62", "v63", "v64", "v65", "v66", "v67", "v68", "v69", "v70", "v71", "v72", "v73", "v74", "v75", "v76", "v77", "v78", "v79", "v80", "v81", "v82", "v83", "v84", "v85", "v86", "v87", "v88", "v89", "v90", "v91", "v92", "v93", "v94", "v95", "v96", "v97", "v98", "v99", "v100", "v101", "v102", "v103", "v104", "v105", "v106", "v107", "v108", "v109", "v110", "v111", "v112", "v113", "v114", "v115", "v116", "v117", "v118", "v119", "v120", "v121", "v122", "v123", "v124", "v125", "v126", "v127", "v128", "v129", "v130", "v131", "v132", "v133", "v134", "v135", "v136", "v137", "v138", "v139", "v140", "v141", "v142", "v143", "v144", "v145", "v146", "v147", "v148", "v149", "v150", "v151", "v152", "v153", "v154", "v155", "v156", "v157", "v158", "v159", "v160", "v161", "v162", "v163", "v164", "v165", "v166", "v167", "v168", "v169", "v170", "v171", "v172", "v173", "v174", "v175", "v176", "v177", "v178", "v179", "v180", "v181", "v182", "v183", "v184", "v185", "v186", "v187", "v188", "v189", "v190", "v191", "v192", "v193", "v194", "v195", "v196", "v197", "v198", "v199", "v200", "v201", "v202", "v203", "v204", "v205", "v206", "v207", "v208", "v209", "v210", "v211", "v212", "v213", "v214", "v215", "v216", "v217", "v218", "v219", "v220", "v221", "v222", "v223", "v224", "v225", "v226", "v227", "v228", "v229", "v230", "v231", "v232", "v233", "v234", "v235", "v236", "v237", "v238", "v239", "v240", "v241", "v242", "v243", "v244", "v245", "v246", "v247", "v248", "v249", "v250", "v251", "v252", "v253", "v254", "v255", "a0", "a1", "a2", "a3", "a4", "a5", "a6", "a7", "a8", "a9", "a10", "a11", "a12", "a13", "a14", "a15", "a16", "a17", "a18", "a19", "a20", "a21", "a22", "a23", "a24", "a25", "a26", "a27", "a28", "a29", "a30", "a31", "a32", "a33", "a34", "a35", "a36", "a37", "a38", "a39", "a40", "a41", "a42", "a43", "a44", "a45", "a46", "a47", "a48", "a49", "a50", "a51", "a52", "a53", "a54", "a55", "a56", "a57", "a58", "a59", "a60", "a61", "a62", "a63", "a64", "a65", "a66", "a67", "a68", "a69", "a70", "a71", "a72", "a73", "a74", "a75", "a76", "a77", "a78", "a79", "a80", "a81", "a82", "a83", "a84", "a85", "a86", "a87", "a88", "a89", "a90", "a91", "a92", "a93", "a94", "a95", "a96", "a97", "a98", "a99", "a100", "a101", "a102", "a103", "a104", "a105", "a106", "a107", "a108", "a109", "a110", "a111", "a112", "a113", "a114", "a115", "a116", "a117", "a118", "a119", "a120", "a121", "a122", "a123", "a124", "a125", "a126", "a127", "a128", "a129", "a130", "a131", "a132", "a133", "a134", "a135", "a136", "a137", "a138", "a139", "a140", "a141", "a142", "a143", "a144", "a145", "a146", "a147", "a148", "a149", "a150", "a151", "a152", "a153", "a154", "a155", "a156", "a157", "a158", "a159", "a160", "a161", "a162", "a163", "a164", "a165", "a166", "a167", "a168", "a169", "a170", "a171", "a172", "a173", "a174", "a175", "a176", "a177", "a178", "a179", "a180", "a181", "a182", "a183", "a184", "a185", "a186", "a187", "a188", "a189", "a190", "a191", "a192", "a193", "a194", "a195", "a196", "a197", "a198", "a199", "a200", "a201", "a202", "a203", "a204", "a205", "a206", "a207", "a208", "a209", "a210", "a211", "a212", "a213", "a214", "a215", "a216", "a217", "a218", "a219", "a220", "a221", "a222", "a223", "a224", "a225", "a226", "a227", "a228", "a229", "a230", "a231", "a232", "a233", "a234", "a235", "a236", "a237", "a238", "a239", "a240", "a241", "a242", "a243", "a244", "a245", "a246", "a247", "a248", "a249", "a250", "a251", "a252", "a253", "a254", "a255", "s60", "s61", "s62", "s63", "scc", "memory");

}

extern "C" void kernel_launch(void* const* d_in, const int* in_sizes, int n_in,
                              void* d_out, int out_size, void* d_ws, size_t ws_size,
                              hipStream_t stream) {
    const unsigned short* x = (const unsigned short*)d_in[0];
    const unsigned short* w = (const unsigned short*)d_in[1];
    unsigned short* out = (unsigned short*)d_out;
    conv_atrous<<<224, 256, 0, stream>>>(x, w, out);
}
```
